```python
import torch

def _softmax(x, axis=-1):
    return torch.softmax(x, dim=axis)
def _gelu(x):
    return torch.nn.functional.gelu(x, approximate="tanh")
def _silu(x):
    return torch.nn.functional.silu(x)
def _sigmoid(x):
    return torch.sigmoid(x)
def _layernorm(x, axis=-1, eps=1e-5):
    m = x.mean(dim=axis, keepdim=True)
    v = x.var(dim=axis, keepdim=True, unbiased=False)
    return (x - m) / torch.sqrt(v + eps)
def _scatter_add(tgt, idx, src, axis=0):
    return tgt.clone().index_add_(axis, idx, src)
def _segment_sum(src, seg, n):
    out = torch.zeros((n,) + src.shape[1:], dtype=src.dtype)
    return out.index_add_(0, seg, src)
def _pad(x, pw):
    flat = tuple(v for pair in reversed(pw) for v in pair)
    return torch.nn.functional.pad(x, flat)

kernel_name = 'gated_mix__compressed_kv_cla__dense_activation_gelu__decode'

def setup_inputs():

    _w = (lambda a: (lambda t: t.to(torch.bfloat16) if t.is_floating_point() else t)(torch.as_tensor(a)))
    def _A_setup():
        B, H, Sq, Sk, D, M = 2, 32, 1, 4096, 128, 576
        Dc = 4 * D
        return {
            "x": _w((lambda *s: torch.randn(*s, dtype=torch.bfloat16) * 0.05)(B, Sq, M)),
            "c_kv": _w((lambda *s: torch.randn(*s, dtype=torch.bfloat16) * 0.8)(B, Sk, Dc)),
            "w_q": _w((lambda *s: torch.randn(*s, dtype=torch.bfloat16) * 6.4)(M, H, D) * (M ** -0.5)),
            "w_uk": _w((lambda *s: torch.randn(*s, dtype=torch.bfloat16) * 6.4)(Dc, H, D) * (Dc ** -0.5)),
            "w_uv": _w((lambda *s: torch.randn(*s, dtype=torch.bfloat16) * 0.8)(Dc, H, D) * (Dc ** -0.5)),
            "w_o": _w((lambda *s: torch.randn(*s, dtype=torch.bfloat16) * 0.8)(H, D, M) * ((H * D) ** -0.5)),
        }
    _A_in = _A_setup()

    def _B_setup():
        B, S, D, F = 2, 1, 576, 4608
        return {
            "x": _w((lambda *s: torch.randn(*s, dtype=torch.bfloat16) * 0.05)(B, S, D)),
            "w1": _w((lambda *s: torch.randn(*s, dtype=torch.bfloat16) * 3.2)(D, F) * (D ** -0.5)),
            "b1": _w((lambda *s: torch.randn(*s, dtype=torch.bfloat16) * 3.2)(F) * 0.01),
            "w2": _w((lambda *s: torch.randn(*s, dtype=torch.bfloat16) * 0.8)(F, D) * (F ** -0.5)),
            "b2": _w((lambda *s: torch.randn(*s, dtype=torch.bfloat16) * 1.6)(D) * 0.01),
        }
    _B_in = _B_setup()

    return {
        "A_x": _A_in["x"],
        "A_c_kv": _A_in["c_kv"],
        "A_w_q": _A_in["w_q"],
        "A_w_uk": _A_in["w_uk"],
        "A_w_uv": _A_in["w_uv"],
        "A_w_o": _A_in["w_o"],
        "B_x": _B_in["x"],
        "B_w1": _B_in["w1"],
        "B_b1": _B_in["b1"],
        "B_w2": _B_in["w2"],
        "B_b2": _B_in["b2"],
        "gate_w": _w((lambda *s: torch.randn(*s, dtype=torch.bfloat16) * 0.05)(576) * 16.0),
    }

def reference(A_x, A_c_kv, A_w_q, A_w_uk, A_w_uv, A_w_o, B_x, B_w1, B_b1, B_w2, B_b2, gate_w):
    def _A_ref(x, c_kv, w_q, w_uk, w_uv, w_o):
        B, Sq, M = x.shape[0], x.shape[1], x.shape[2]
        Sk = c_kv.shape[1]
        H, D = w_q.shape[1], w_q.shape[2]
        q = torch.einsum("bqm,mhd->bhqd", x, w_q)
        k = torch.einsum("bkc,chd->bhkd", c_kv, w_uk)
        v = torch.einsum("bkc,chd->bhkd", c_kv, w_uv)
        scores = torch.einsum("bhqd,bhkd->bhqk", q, k) * (D ** -0.5)
        off = Sk - Sq
        qi = torch.arange(Sq)[:, None]
        kj = torch.arange(Sk)[None, :]
        cmask = kj <= (qi + off)
        scores = torch.where(cmask[None, None], scores, -1e9)
        p = _softmax(scores, axis=-1)
        ctx = torch.einsum("bhqk,bhkd->bhqd", p, v)
        return torch.einsum("bhqd,hdm->bqm", ctx, w_o)

    def _B_ref(x, w1, b1, w2, b2):
        h = torch.einsum("bsd,df->bsf", x, w1) + b1
        h = _gelu(h)
        return torch.einsum("bsf,fd->bsd", h, w2) + b2

    x = A_x
    g = _sigmoid((x * gate_w).sum(-1, keepdims=True))
    a = _A_ref(x, A_c_kv, A_w_q, A_w_uk, A_w_uv, A_w_o)
    b = _B_ref(x, B_w1, B_b1, B_w2, B_b2)
    return g * a + (1 - g) * b
```

```cpp
#include <hip/hip_runtime.h>
#include <hip/hip_bf16.h>
#include <stdint.h>

typedef unsigned short u16;
__device__ __forceinline__ float bf2f(u16 v) { return __uint_as_float(((unsigned)v) << 16); }
__device__ __forceinline__ u16 f2bf(float f) { __hip_bfloat16 h = __float2bfloat16(f); return *(u16*)&h; }

constexpr int B = 2, H = 32, SK = 4096, D = 128, M = 576, DC = 512, F = 4608;
constexpr int KS = 64;
constexpr int KPS = SK / KS;

constexpr size_t OFF_QABS = 0;
constexpr size_t OFF_PART = OFF_QABS + B * H * DC;
constexpr size_t OFF_ML = OFF_PART + (size_t)B * KS * H * DC;
constexpr size_t OFF_OA = OFF_ML + B * KS * H * 2;
constexpr size_t OFF_OB = OFF_OA + H * B * M;

__global__ void __launch_bounds__(256) k1(const u16* x, const u16* wq, const u16* wuk, const u16* w1, const u16* b1, const u16* w2, float* ws) {
    __shared__ float xs[B][M];
    __shared__ float red[B][256];
    __shared__ float qs[B][128];
    int tid = threadIdx.x;
    for (int i = tid; i < B * M; i += 256) xs[i / M][i % M] = bf2f(x[i]);
    __syncthreads();
    if (blockIdx.x < H) {
        int h = blockIdx.x;
        int d = tid & 127, half = tid >> 7;
        float a0 = 0, a1 = 0;
        for (int m = half; m < M; m += 2) { float w = bf2f(wq[(size_t)m * H * D + h * D + d]); a0 += xs[0][m] * w; a1 += xs[1][m] * w; }
        red[0][tid] = a0; red[1][tid] = a1;
        __syncthreads();
        if (tid < 128) { qs[0][tid] = bf2f(f2bf(red[0][tid] + red[0][tid + 128])); qs[1][tid] = bf2f(f2bf(red[1][tid] + red[1][tid + 128])); }
        __syncthreads();
        for (int c = tid; c < DC; c += 256) {
            float s0 = 0, s1 = 0;
            for (int dd = 0; dd < D; ++dd) { float w = bf2f(wuk[(size_t)c * H * D + h * D + dd]); s0 += qs[0][dd] * w; s1 += qs[1][dd] * w; }
            ws[OFF_QABS + (0 * H + h) * DC + c] = s0;
            ws[OFF_QABS + (1 * H + h) * DC + c] = s1;
        }
    } else {
        int fb = blockIdx.x - H;
        int f = tid & 63, part = tid >> 6;
        float a0 = 0, a1 = 0;
        for (int m = part; m < M; m += 4) { float w = bf2f(w1[(size_t)m * F + fb * 64 + f]); a0 += xs[0][m] * w; a1 += xs[1][m] * w; }
        red[0][tid] = a0; red[1][tid] = a1;
        __syncthreads();
        if (tid < 128) {
            int b = tid >> 6, ff = tid & 63;
            float s = red[b][ff] + red[b][ff + 64] + red[b][ff + 128] + red[b][ff + 192];
            s = bf2f(f2bf(s));
            s = bf2f(f2bf(s + bf2f(b1[fb * 64 + ff])));
            float t = tanhf(0.7978845608028654f * (s + 0.044715f * s * s * s));
            qs[b][ff] = bf2f(f2bf(0.5f * s * (1.f + t)));
        }
        __syncthreads();
        for (int m = tid; m < M; m += 256) {
            float s0 = 0, s1 = 0;
            for (int ff = 0; ff < 64; ++ff) { float w = bf2f(w2[(size_t)(fb * 64 + ff) * M + m]); s0 += qs[0][ff] * w; s1 += qs[1][ff] * w; }
            ws[OFF_OB + (fb * B + 0) * M + m] = s0;
            ws[OFF_OB + (fb * B + 1) * M + m] = s1;
        }
    }
}

__global__ void __launch_bounds__(256) k2(const u16* ckv, float* ws) {
    __shared__ float qa[H][DC + 1];
    __shared__ u16 cs[KPS][DC + 2];
    __shared__ float sc[H][KPS];
    int b = blockIdx.x / KS, ks = blockIdx.x % KS, tid = threadIdx.x;
    for (int i = tid; i < H * DC; i += 256) qa[i / DC][i % DC] = ws[OFF_QABS + (size_t)b * H * DC + i];
    for (int i = tid; i < KPS * DC; i += 256) cs[i / DC][i % DC] = ckv[((size_t)b * SK + ks * KPS) * DC + i];
    __syncthreads();
    for (int i = tid; i < H * KPS; i += 256) {
        int h = i / KPS, k = i % KPS;
        float s = 0;
        for (int c = 0; c < DC; ++c) s += qa[h][c] * bf2f(cs[k][c]);
        sc[h][k] = s * 0.08838834764831845f;
    }
    __syncthreads();
    if (tid < H) {
        float m = -INFINITY;
        for (int k = 0; k < KPS; ++k) m = fmaxf(m, sc[tid][k]);
        float l = 0;
        for (int k = 0; k < KPS; ++k) { float p = __expf(sc[tid][k] - m); sc[tid][k] = p; l += p; }
        ws[OFF_ML + ((b * KS + ks) * H + tid) * 2] = m;
        ws[OFF_ML + ((b * KS + ks) * H + tid) * 2 + 1] = l;
    }
    __syncthreads();
    for (int i = tid; i < H * DC; i += 256) {
        int h = i / DC, c = i % DC;
        float s = 0;
        for (int k = 0; k < KPS; ++k) s += sc[h][k] * bf2f(cs[k][c]);
        ws[OFF_PART + ((size_t)(b * KS + ks) * H + h) * DC + c] = s;
    }
}

__global__ void __launch_bounds__(256) k3(const u16* wuv, const u16* wo, float* ws) {
    __shared__ float cc[B][DC];
    __shared__ float wgt[B][KS];
    __shared__ float red[B][256];
    __shared__ float ctx[B][D];
    int h = blockIdx.x, tid = threadIdx.x;
    if (tid < B) {
        int b = tid;
        float m = -INFINITY;
        for (int s = 0; s < KS; ++s) m = fmaxf(m, ws[OFF_ML + ((b * KS + s) * H + h) * 2]);
        float l = 0;
        for (int s = 0; s < KS; ++s) { float w = __expf(ws[OFF_ML + ((b * KS + s) * H + h) * 2] - m); wgt[b][s] = w; l += w * ws[OFF_ML + ((b * KS + s) * H + h) * 2 + 1]; }
        for (int s = 0; s < KS; ++s) wgt[b][s] /= l;
    }
    __syncthreads();
    for (int i = tid; i < B * DC; i += 256) {
        int b = i / DC, c = i % DC;
        float s = 0;
        for (int k = 0; k < KS; ++k) s += wgt[b][k] * ws[OFF_PART + ((size_t)(b * KS + k) * H + h) * DC + c];
        cc[b][c] = s;
    }
    __syncthreads();
    {
        int d = tid & 127, half = tid >> 7;
        float a0 = 0, a1 = 0;
        for (int c = half; c < DC; c += 2) { float w = bf2f(wuv[(size_t)c * H * D + h * D + d]); a0 += cc[0][c] * w; a1 += cc[1][c] * w; }
        red[0][tid] = a0; red[1][tid] = a1;
        __syncthreads();
        if (tid < 128) { ctx[0][tid] = bf2f(f2bf(red[0][tid] + red[0][tid + 128])); ctx[1][tid] = bf2f(f2bf(red[1][tid] + red[1][tid + 128])); }
        __syncthreads();
    }
    for (int m = tid; m < M; m += 256) {
        float s0 = 0, s1 = 0;
        for (int d = 0; d < D; ++d) { float w = bf2f(wo[((size_t)h * D + d) * M + m]); s0 += ctx[0][d] * w; s1 += ctx[1][d] * w; }
        ws[OFF_OA + (h * B + 0) * M + m] = s0;
        ws[OFF_OA + (h * B + 1) * M + m] = s1;
    }
}

__global__ void __launch_bounds__(256) k4(const u16* x, const u16* gw, const u16* b2, const float* ws, u16* out) {
    __shared__ float g[B];
    int tid = threadIdx.x;
    if (tid < B) {
        float s = 0;
        for (int m = 0; m < M; ++m) s += bf2f(f2bf(bf2f(x[tid * M + m]) * bf2f(gw[m])));
        s = bf2f(f2bf(s));
        g[tid] = bf2f(f2bf(1.f / (1.f + __expf(-s))));
    }
    __syncthreads();
    int i = blockIdx.x * 256 + tid;
    if (i < B * M) {
        int b = i / M, m = i % M;
        float a = 0, bb = 0;
        for (int h = 0; h < H; ++h) a += ws[OFF_OA + (h * B + b) * M + m];
        for (int f = 0; f < 72; ++f) bb += ws[OFF_OB + (f * B + b) * M + m];
        a = bf2f(f2bf(a));
        bb = bf2f(f2bf(bf2f(f2bf(bb)) + bf2f(b2[m])));
        float gg = g[b];
        float t1 = bf2f(f2bf(gg * a));
        float og = bf2f(f2bf(1.f - gg));
        float t2 = bf2f(f2bf(og * bb));
        out[i] = f2bf(t1 + t2);
    }
}

extern "C" void kernel_launch(void* const* d_in, const int* in_sizes, int n_in,
                              void* d_out, int out_size, void* d_ws, size_t ws_size,
                              hipStream_t stream) {
    const u16* x = (const u16*)d_in[0];
    const u16* ckv = (const u16*)d_in[1];
    const u16* wq = (const u16*)d_in[2];
    const u16* wuk = (const u16*)d_in[3];
    const u16* wuv = (const u16*)d_in[4];
    const u16* wo = (const u16*)d_in[5];
    const u16* w1 = (const u16*)d_in[7];
    const u16* b1 = (const u16*)d_in[8];
    const u16* w2 = (const u16*)d_in[9];
    const u16* b2 = (const u16*)d_in[10];
    const u16* gw = (const u16*)d_in[11];
    float* ws = (float*)d_ws;
    k1<<<H + 72, 256, 0, stream>>>(x, wq, wuk, w1, b1, w2, ws);
    k2<<<B * KS, 256, 0, stream>>>(ckv, ws);
    k3<<<H, 256, 0, stream>>>(wuv, wo, ws);
    k4<<<5, 256, 0, stream>>>(x, gw, b2, ws, (u16*)d_out);
}
```

```cpp
#include <hip/hip_runtime.h>
#include <hip/hip_bf16.h>
#include <stdint.h>

typedef unsigned short u16;
__device__ __forceinline__ float bf2f(u16 v) { return __uint_as_float(((unsigned)v) << 16); }
__device__ __forceinline__ u16 f2bf(float f) { __hip_bfloat16 h = __float2bfloat16(f); return *(u16*)&h; }

constexpr int B = 2, H = 32, SK = 4096, D = 128, M = 576, DC = 512, F = 4608;
constexpr int KS = 64;
constexpr int KPS = SK / KS;

constexpr size_t OFF_QABS = 0;
constexpr size_t OFF_PART = OFF_QABS + B * H * DC;
constexpr size_t OFF_ML = OFF_PART + (size_t)B * KS * H * DC;
constexpr size_t OFF_OA = OFF_ML + B * KS * H * 2;
constexpr size_t OFF_OB = OFF_OA + H * B * M;

__global__ void __launch_bounds__(256) k1(const u16* x, const u16* wq, const u16* wuk, const u16* w1, const u16* b1, const u16* w2, float* ws) {
    __shared__ float xs[B][M];
    __shared__ float red[B][256];
    __shared__ float qs[B][128];
    int tid = threadIdx.x;
    for (int i = tid; i < B * M; i += 256) xs[i / M][i % M] = bf2f(x[i]);
    __syncthreads();
    if (blockIdx.x < H) {
        int h = blockIdx.x;
        int d = tid & 127, half = tid >> 7;
        float a0 = 0, a1 = 0;
        for (int m = half; m < M; m += 2) { float w = bf2f(wq[(size_t)m * H * D + h * D + d]); a0 += xs[0][m] * w; a1 += xs[1][m] * w; }
        red[0][tid] = a0; red[1][tid] = a1;
        __syncthreads();
        if (tid < 128) { qs[0][tid] = bf2f(f2bf(red[0][tid] + red[0][tid + 128])); qs[1][tid] = bf2f(f2bf(red[1][tid] + red[1][tid + 128])); }
        __syncthreads();
        for (int c = tid; c < DC; c += 256) {
            float s0 = 0, s1 = 0;
            for (int dd = 0; dd < D; ++dd) { float w = bf2f(wuk[(size_t)c * H * D + h * D + dd]); s0 += qs[0][dd] * w; s1 += qs[1][dd] * w; }
            ws[OFF_QABS + (0 * H + h) * DC + c] = s0;
            ws[OFF_QABS + (1 * H + h) * DC + c] = s1;
        }
    } else {
        int fb = blockIdx.x - H;
        int f = tid & 63, part = tid >> 6;
        float a0 = 0, a1 = 0;
        for (int m = part; m < M; m += 4) { float w = bf2f(w1[(size_t)m * F + fb * 64 + f]); a0 += xs[0][m] * w; a1 += xs[1][m] * w; }
        red[0][tid] = a0; red[1][tid] = a1;
        __syncthreads();
        if (tid < 128) {
            int b = tid >> 6, ff = tid & 63;
            float s = red[b][ff] + red[b][ff + 64] + red[b][ff + 128] + red[b][ff + 192];
            s = bf2f(f2bf(s));
            s = bf2f(f2bf(s + bf2f(b1[fb * 64 + ff])));
            float t = tanhf(0.7978845608028654f * (s + 0.044715f * s * s * s));
            qs[b][ff] = bf2f(f2bf(0.5f * s * (1.f + t)));
        }
        __syncthreads();
        for (int m = tid; m < M; m += 256) {
            float s0 = 0, s1 = 0;
            for (int ff = 0; ff < 64; ++ff) { float w = bf2f(w2[(size_t)(fb * 64 + ff) * M + m]); s0 += qs[0][ff] * w; s1 += qs[1][ff] * w; }
            ws[OFF_OB + (fb * B + 0) * M + m] = s0;
            ws[OFF_OB + (fb * B + 1) * M + m] = s1;
        }
    }
}

__global__ void __launch_bounds__(256) k2(const u16* ckv, float* ws) {
    __shared__ float qa[H][DC + 1];
    __shared__ u16 cs[KPS][DC + 2];
    __shared__ float sc[H][KPS];
    int b = blockIdx.x / KS, ks = blockIdx.x % KS, tid = threadIdx.x;
    for (int i = tid; i < H * DC; i += 256) qa[i / DC][i % DC] = ws[OFF_QABS + (size_t)b * H * DC + i];
    for (int i = tid; i < KPS * DC; i += 256) cs[i / DC][i % DC] = ckv[((size_t)b * SK + ks * KPS) * DC + i];
    __syncthreads();
    for (int i = tid; i < H * KPS; i += 256) {
        int h = i / KPS, k = i % KPS;
        float s = 0;
        for (int c = 0; c < DC; ++c) s += qa[h][c] * bf2f(cs[k][c]);
        sc[h][k] = s * 0.08838834764831845f;
    }
    __syncthreads();
    if (tid < H) {
        float m = -INFINITY;
        for (int k = 0; k < KPS; ++k) m = fmaxf(m, sc[tid][k]);
        float l = 0;
        for (int k = 0; k < KPS; ++k) { float p = __expf(sc[tid][k] - m); sc[tid][k] = p; l += p; }
        ws[OFF_ML + ((b * KS + ks) * H + tid) * 2] = m;
        ws[OFF_ML + ((b * KS + ks) * H + tid) * 2 + 1] = l;
    }
    __syncthreads();
    for (int i = tid; i < H * DC; i += 256) {
        int h = i / DC, c = i % DC;
        float s = 0;
        for (int k = 0; k < KPS; ++k) s += sc[h][k] * bf2f(cs[k][c]);
        ws[OFF_PART + ((size_t)(b * KS + ks) * H + h) * DC + c] = s;
    }
}

__global__ void __launch_bounds__(256) k3(const u16* wuv, const u16* wo, float* ws) {
    __shared__ float cc[B][DC];
    __shared__ float wgt[B][KS];
    __shared__ float red[B][256];
    __shared__ float ctx[B][D];
    int h = blockIdx.x, tid = threadIdx.x;
    if (tid < B) {
        int b = tid;
        float m = -INFINITY;
        for (int s = 0; s < KS; ++s) m = fmaxf(m, ws[OFF_ML + ((b * KS + s) * H + h) * 2]);
        float l = 0;
        for (int s = 0; s < KS; ++s) { float w = __expf(ws[OFF_ML + ((b * KS + s) * H + h) * 2] - m); wgt[b][s] = w; l += w * ws[OFF_ML + ((b * KS + s) * H + h) * 2 + 1]; }
        for (int s = 0; s < KS; ++s) wgt[b][s] /= l;
    }
    __syncthreads();
    for (int i = tid; i < B * DC; i += 256) {
        int b = i / DC, c = i % DC;
        float s = 0;
        for (int k = 0; k < KS; ++k) s += wgt[b][k] * ws[OFF_PART + ((size_t)(b * KS + k) * H + h) * DC + c];
        cc[b][c] = s;
    }
    __syncthreads();
    {
        int d = tid & 127, half = tid >> 7;
        float a0 = 0, a1 = 0;
        for (int c = half; c < DC; c += 2) { float w = bf2f(wuv[(size_t)c * H * D + h * D + d]); a0 += cc[0][c] * w; a1 += cc[1][c] * w; }
        red[0][tid] = a0; red[1][tid] = a1;
        __syncthreads();
        if (tid < 128) { ctx[0][tid] = bf2f(f2bf(red[0][tid] + red[0][tid + 128])); ctx[1][tid] = bf2f(f2bf(red[1][tid] + red[1][tid + 128])); }
        __syncthreads();
    }
    for (int m = tid; m < M; m += 256) {
        float s0 = 0, s1 = 0;
        for (int d = 0; d < D; ++d) { float w = bf2f(wo[((size_t)h * D + d) * M + m]); s0 += ctx[0][d] * w; s1 += ctx[1][d] * w; }
        ws[OFF_OA + (h * B + 0) * M + m] = s0;
        ws[OFF_OA + (h * B + 1) * M + m] = s1;
    }
}

__global__ void __launch_bounds__(256) k4(const u16* x, const u16* gw, const u16* b2, const float* ws, u16* out) {
    __shared__ float g[B];
    int tid = threadIdx.x;
    if (tid < B) {
        float s = 0;
        for (int m = 0; m < M; ++m) s += bf2f(f2bf(bf2f(x[tid * M + m]) * bf2f(gw[m])));
        s = bf2f(f2bf(s));
        g[tid] = bf2f(f2bf(1.f / (1.f + __expf(-s))));
    }
    __syncthreads();
    int i = blockIdx.x * 256 + tid;
    if (i < B * M) {
        int b = i / M, m = i % M;
        float a = 0, bb = 0;
        for (int h = 0; h < H; ++h) a += ws[OFF_OA + (h * B + b) * M + m];
        for (int f = 0; f < 72; ++f) bb += ws[OFF_OB + (f * B + b) * M + m];
        a = bf2f(f2bf(a));
        bb = bf2f(f2bf(bf2f(f2bf(bb)) + bf2f(b2[m])));
        float gg = g[b];
        float t1 = bf2f(f2bf(gg * a));
        float og = bf2f(f2bf(1.f - gg));
        float t2 = bf2f(f2bf(og * bb));
        out[i] = f2bf(t1 + t2);
    }
}


typedef unsigned v4u __attribute__((ext_vector_type(4)));
typedef unsigned long long u64;
#define GAS __attribute__((address_space(1)))
__device__ __forceinline__ u64 rt() { u64 t; asm volatile("s_memrealtime %0\n\ts_waitcnt lgkmcnt(0)" : "=s"(t) :: "memory"); return t; }
constexpr size_t OFF_ST = 32u << 20;
struct PP { const v4u* p[7]; };
template <int NTMODE>
__global__ void __launch_bounds__(512) k0(PP pp, float* ws) {
    u64 t0 = rt();
    const int tid = threadIdx.x, wg = blockIdx.x;
    const int g = wg * 512 + tid;
    v4u r[18];
    const int np[7] = {524288, 294912, 262144, 262144, 294912, 331776, 331776};
    int k = 0;
    unsigned a = 0;
    if (NTMODE >= 0) {
#pragma unroll
    for (int j = 0; j < 7; ++j) {
#pragma unroll
        for (int i = 0; i < (np[j] + 131071) / 131072; ++i) {
            int idx = i * 131072 + g; if (idx >= np[j]) idx = g;
            const GAS v4u* p = (const GAS v4u*)pp.p[j] + idx;
            v4u v = NTMODE == 1 ? __builtin_nontemporal_load(p) : *p;
            a ^= v.x ^ v.y ^ v.z ^ v.w;
        }
    }
    }
    asm volatile("" :: "v"(a));
    u64 t1 = rt();
    u64* st = (u64*)(ws + OFF_ST);
    if ((tid & 63) == 0) { st[(wg * 8 + (tid >> 6)) * 2] = t0; st[(wg * 8 + (tid >> 6)) * 2 + 1] = t1 + (a == 0x12345678); }
}
template <int WHAT, int MULT>
__global__ void kspin(const float* ws) {
    const u64* st = (const u64*)(ws + OFF_ST);
    u64 smin = ~0ull, smax = 0, emax = 0;
    for (int i = 0; i < 2048; ++i) { u64 s = st[2 * i], e = st[2 * i + 1]; smin = s < smin ? s : smin; smax = s > smax ? s : smax; emax = e > emax ? e : emax; }
    u64 span = WHAT == 0 ? (smax - smin) : WHAT == 1 ? (emax - smin) : (emax - smax);
    if (span > 5000) span = 5000;
    u64 t = rt();
    while (rt() < t + span * MULT) __builtin_amdgcn_s_sleep(8);
}

extern "C" void kernel_launch(void* const* d_in, const int* in_sizes, int n_in,
                              void* d_out, int out_size, void* d_ws, size_t ws_size,
                              hipStream_t stream) {
    const u16* x = (const u16*)d_in[0];
    const u16* ckv = (const u16*)d_in[1];
    const u16* wq = (const u16*)d_in[2];
    const u16* wuk = (const u16*)d_in[3];
    const u16* wuv = (const u16*)d_in[4];
    const u16* wo = (const u16*)d_in[5];
    const u16* w1 = (const u16*)d_in[7];
    const u16* b1 = (const u16*)d_in[8];
    const u16* w2 = (const u16*)d_in[9];
    const u16* b2 = (const u16*)d_in[10];
    const u16* gw = (const u16*)d_in[11];
    float* ws = (float*)d_ws;
    PP pp; pp.p[0] = (const v4u*)ckv; pp.p[1] = (const v4u*)wq; pp.p[2] = (const v4u*)wuk; pp.p[3] = (const v4u*)wuv; pp.p[4] = (const v4u*)wo; pp.p[5] = (const v4u*)w1; pp.p[6] = (const v4u*)w2;
    k0<0><<<256, 512, 0, stream>>>(pp, ws);
    kspin<1, 0><<<1, 1, 0, stream>>>(ws);
    k1<<<H + 72, 256, 0, stream>>>(x, wq, wuk, w1, b1, w2, ws);
    k2<<<B * KS, 256, 0, stream>>>(ckv, ws);
    k3<<<H, 256, 0, stream>>>(wuv, wo, ws);
    k4<<<5, 256, 0, stream>>>(x, gw, b2, ws, (u16*)d_out);
}
```

```cpp
#include <hip/hip_runtime.h>
#include <hip/hip_bf16.h>
#include <stdint.h>

typedef unsigned short u16;
typedef unsigned long long u64;
typedef unsigned v4u __attribute__((ext_vector_type(4)));
typedef unsigned v2u __attribute__((ext_vector_type(2)));
typedef int v4i __attribute__((ext_vector_type(4)));
typedef float f32x4 __attribute__((ext_vector_type(4)));
typedef float f32x2 __attribute__((ext_vector_type(2)));
typedef short s16x4 __attribute__((ext_vector_type(4)));
typedef __bf16 bf16x8 __attribute__((ext_vector_type(8)));
typedef __bf16 bf16x2 __attribute__((ext_vector_type(2)));
#define GAS __attribute__((address_space(1)))
#define LAS __attribute__((address_space(3)))
#define DI __device__ __forceinline__
typedef __amdgpu_buffer_rsrc_t rsrc_t;

constexpr int NT = 512;
constexpr unsigned SENT = 0xFFFFFFFFu;
constexpr int SC1 = 16;
constexpr unsigned SPIN_CAP = 1u << 17;
constexpr u64 TAG_STEP = 0x9E3779B97F4A7C16ull;

constexpr int WS_E = 0;
constexpr int WS_P1 = 4096;
constexpr int WS_F2 = WS_P1 + 16384;
constexpr int WS_R3 = WS_F2 + 16384;
constexpr int WS_H1 = WS_R3 + 16384;
constexpr int WS_QA = WS_H1 + 128 * 512;
constexpr int WS_M1 = WS_QA + 65536;
constexpr int WS_ML = WS_M1 + 128 * 1152;
constexpr int WS_H3 = WS_ML + 16384;
constexpr int WS_H5 = WS_H3 + 64 * 32768;
constexpr int WS_END = WS_H5 + 256 * 4608;

constexpr int LA_WQ = 0, LA_WUK = 36864, LA_WO = 0, LA_W1 = 73728, LA_WUV = 115200, LA_CMB = 147968, LA_CTX = 156160, LA_COL = 156416;
constexpr int LB_CKV = 0, LB_W2 = 0, LB_Q = 131072, LB_P = 147456, LB_MX = 151552, LB_L = 152064, LB_H = 152576;
constexpr int LDS_BYTES = 156672;

#define STAMP(i) do {} while (0)

DI v4i mk_srd(const void* p, unsigned bytes) {
    const u64 a = (u64)p;
    return (v4i){(int)(unsigned)a, (int)((unsigned)(a >> 32) & 0xFFFFu), (int)bytes, 0x00020000};
}
DI void dma16(v4i srd, unsigned voff, unsigned soff, unsigned lds) {
    asm volatile("s_mov_b32 m0, %0\n\ts_nop 0\n\tbuffer_load_dwordx4 %1, %2, %3 offen lds" :: "s"(lds), "v"(voff), "s"(srd), "s"(soff) : "memory");
}
DI v4u ald128(v4i srd, unsigned voff) { v4u r; asm volatile("buffer_load_dwordx4 %0, %1, %2, 0 offen sc1" : "=v"(r) : "v"(voff), "s"(srd) : "memory"); return r; }
DI v4u ald128p(v4i srd, unsigned voff) { v4u r; asm volatile("buffer_load_dwordx4 %0, %1, %2, 0 offen" : "=v"(r) : "v"(voff), "s"(srd) : "memory"); return r; }
DI v2u ald64p(v4i srd, unsigned voff) { v2u r; asm volatile("buffer_load_dwordx2 %0, %1, %2, 0 offen" : "=v"(r) : "v"(voff), "s"(srd) : "memory"); return r; }
DI v2u ald_e(v4i srd) { v2u r; asm volatile("buffer_load_dwordx2 %0, off, %1, 0 sc1" : "=v"(r) : "s"(srd) : "memory"); return r; }
#define WAIT_VM(N, ...) asm volatile("s_waitcnt vmcnt(" #N ")" : __VA_ARGS__ :: "memory")
DI v2u ald64(v4i srd, unsigned voff) { v2u r; asm volatile("buffer_load_dwordx2 %0, %1, %2, 0 offen sc1" : "=v"(r) : "v"(voff), "s"(srd) : "memory"); return r; }
DI bool flags_ok(v2u f, v2u tag) { return __all((f.x == tag.x) & (f.y == tag.y)); }
DI bool flags_ok2(v2u f, v2u tag) { return __all(((f.x | 1u) == (tag.x | 1u)) & (f.y == tag.y)); }
DI v2u mk_tag(v2u e) { const u64 t = (((u64)e.y << 32) | e.x) + TAG_STEP; return (v2u){(unsigned)t & ~1u, (unsigned)(t >> 32)}; }
template <int N> DI void wait_vm() { asm volatile("s_waitcnt vmcnt(%0)" :: "i"(N) : "memory"); }
DI void barrier_lds() { asm volatile("s_waitcnt lgkmcnt(0)\n\ts_barrier" ::: "memory"); }
DI f32x4 mfma32(v4u a, v4u b, f32x4 c) { return __builtin_amdgcn_mfma_f32_16x16x32_bf16(__builtin_bit_cast(bf16x8, a), __builtin_bit_cast(bf16x8, b), c, 0, 0, 0); }
DI f32x4 mfma16(v2u a, v2u b, f32x4 c) { return mfma32((v4u){a.x, a.y, 0u, 0u}, (v4u){b.x, b.y, 0u, 0u}, c); }
DI v2u lds_tr(LAS char* L, unsigned off) { return __builtin_bit_cast(v2u, __builtin_amdgcn_ds_read_tr16_b64_v4i16((LAS s16x4*)(L + off))); }
DI v4u lds_tr2(LAS char* L, unsigned o0, unsigned o1) { const v2u a = lds_tr(L, o0), b = lds_tr(L, o1); return (v4u){a.x, a.y, b.x, b.y}; }
DI v4u lds128(LAS char* L, unsigned off) { return *(LAS v4u*)(L + off); }
DI unsigned pk(float lo, float hi) { return __builtin_bit_cast(unsigned, __builtin_convertvector((f32x2){lo, hi}, bf16x2)); }
DI unsigned nos(unsigned v) { return v < 0xFFFFFFFEu ? v : 0xFFFFFFFEu; }
DI float bflo(unsigned v) { return __uint_as_float(v << 16); }
DI float bfhi(unsigned v) { return __uint_as_float(v & 0xFFFF0000u); }
DI float bfr(float f) { return bflo(pk(f, f)); }
template <int CTRL> DI float dpp(float x) { return __builtin_bit_cast(float, __builtin_amdgcn_mov_dpp(__builtin_bit_cast(int, x), CTRL, 0xf, 0xf, true)); }
constexpr int XOR1 = 0xB1, XOR2 = 0x4E, ROR4 = 0x124, ROR8 = 0x128;
DI float x16max(float x) { auto s = __builtin_amdgcn_permlane16_swap(__float_as_uint(x), __float_as_uint(x), false, false); return fmaxf(__uint_as_float(s[0]), __uint_as_float(s[1])); }
DI float x32max(float x) { auto s = __builtin_amdgcn_permlane32_swap(__float_as_uint(x), __float_as_uint(x), false, false); return fmaxf(__uint_as_float(s[0]), __uint_as_float(s[1])); }
DI float x16sum(float x) { auto s = __builtin_amdgcn_permlane16_swap(__float_as_uint(x), __float_as_uint(x), false, false); return __uint_as_float(s[0]) + __uint_as_float(s[1]); }
DI float x32sum(float x) { auto s = __builtin_amdgcn_permlane32_swap(__float_as_uint(x), __float_as_uint(x), false, false); return __uint_as_float(s[0]) + __uint_as_float(s[1]); }
DI unsigned umax2(unsigned a, unsigned b) { return a > b ? a : b; }
DI unsigned umax4(v4u v) { return umax2(umax2(v.x, v.y), umax2(v.z, v.w)); }
DI void st128(rsrc_t rs, v4u v, unsigned off) { __builtin_amdgcn_raw_buffer_store_b128(v, rs, off, 0, SC1); }
DI void st64(rsrc_t rs, v2u v, unsigned off) { __builtin_amdgcn_raw_buffer_store_b64(v, rs, off, 0, SC1); }
DI v4u ld128(rsrc_t rs, unsigned off) { return __builtin_amdgcn_raw_buffer_load_b128(rs, off, 0, SC1); }
DI v2u ld64(rsrc_t rs, unsigned off) { return __builtin_amdgcn_raw_buffer_load_b64(rs, off, 0, SC1); }
DI unsigned ld32(rsrc_t rs, unsigned off) { return __builtin_amdgcn_raw_buffer_load_b32(rs, off, 0, SC1); }

template <int SLP, bool ANY = false> DI void wait_flags(rsrc_t rs, unsigned off, v2u tag) {
#pragma unroll 1
    for (unsigned spins = 0; spins < SPIN_CAP; ++spins) {
        asm volatile("" ::: "memory");
        const v2u f = ld64(rs, off);
        if (ANY ? flags_ok2(f, tag) : flags_ok(f, tag)) break;
        if (SLP) __builtin_amdgcn_s_sleep(SLP);
    }
    asm volatile("" ::: "memory");
}
template <int N> DI bool landed(const v4u (&r)[N]) {
    unsigned m = 0;
#pragma unroll
    for (int i = 0; i < N; ++i) m = umax2(m, umax4(r[i]));
    return !__any(m == SENT);
}
template <int N, int SLP = 0> DI void gather(rsrc_t rs, const unsigned (&off)[N], v4u (&r)[N]) {
#pragma unroll 1
    for (unsigned spins = 0;; ++spins) {
        asm volatile("" ::: "memory");
#pragma unroll
        for (int i = 0; i < N; ++i) r[i] = ld128(rs, off[i]);
        unsigned m = 0;
#pragma unroll
        for (int i = 0; i < N; ++i) m = umax2(m, umax4(r[i]));
        if (!__any(m == SENT) || spins >= SPIN_CAP) break;
        if (SLP) __builtin_amdgcn_s_sleep(SLP);
    }
}

template <int NS32, bool TAIL16>
DI void proj576_load(LAS char* L, int base, int wave, int lane, v4u (&wf)[5][NS32], v2u (&wt)[5]) {
    const int g = lane >> 4, q = (lane >> 2) & 3, p = lane & 3, tt = wave & 3, g0 = wave >> 2;
    const int x = (q >> 1) | ((g & 1) << 1);
    const unsigned a0 = base + g * 9216 + g0 * 1024 + q * 128 + 16 * ((2 * tt + (p >> 1)) ^ (x << 1)) + 8 * (p & 1);
    const unsigned at = base + 4 * NS32 * 9216 + g0 * 1024 + q * 128 + 16 * ((2 * tt + (p >> 1)) ^ ((q >> 1) << 1)) + 8 * (p & 1);
#pragma unroll
    for (int i = 0; i < 5; ++i) {
        if (i < 4 || g0 == 0) {
#pragma unroll
            for (int s = 0; s < NS32; ++s) wf[i][s] = lds_tr2(L, a0 + s * 36864 + i * 2048, a0 + s * 36864 + i * 2048 + 512);
            if (TAIL16) wt[i] = lds_tr(L, at + i * 2048);
        }
    }
}
template <int NS32, bool TAIL16>
DI void proj576_run(rsrc_t ws, int bid, int wave, int lane, const v4u (&wf)[5][NS32], const v2u (&wt)[5], const v4u (&bf)[NS32], v2u bt) {
    const int g = lane >> 4, n = lane & 15, tt = wave & 3, g0 = wave >> 2;
    const unsigned o0 = WS_H5 + bid * 4608 + (n & 1) * 2304 + (g0 * 64 + tt * 16 + 4 * g) * 4;
#pragma unroll
    for (int i = 0; i < 5; ++i) {
        if (i < 4 || g0 == 0) {
            f32x4 c = {0.f, 0.f, 0.f, 0.f};
#pragma unroll
            for (int s = 0; s < NS32; ++s) c = mfma32(wf[i][s], bf[s], c);
            if (TAIL16) c = mfma16(wt[i], bt, c);
            const v4u v = {nos(__float_as_uint(c[0])), nos(__float_as_uint(c[1])), nos(__float_as_uint(c[2])), nos(__float_as_uint(c[3]))};
            if (n < 2) st128(ws, v, o0 + i * 512);
        }
    }
}

#define WO_BEFORE 4
#define WO_MID 9
#define A_DELAY 0
#define H1_SLP 2
#define Q_SLP 4
#define FA_AT 10
#define B_DELAY 16
#define Q_DELAY 0
#define H1_DELAY 0
#define CODE_PF 1
__global__ void __launch_bounds__(NT, 2)
fused_decode_kernel(const u16* __restrict__ xp, const u16* __restrict__ ckv, const u16* __restrict__ wq, const u16* __restrict__ wuk,
                    const u16* __restrict__ wuv, const u16* __restrict__ wo, const u16* __restrict__ w1, const u16* __restrict__ b1,
                    const u16* __restrict__ w2, const u16* __restrict__ b2, const u16* __restrict__ gw, u16* __restrict__ out, char* wsp) {
    __shared__ __attribute__((aligned(1024))) char smem[LDS_BYTES];
    LAS char* L = (LAS char*)smem;
    const unsigned lds0 = (unsigned)(size_t)L;
    STAMP(0);
    u64 pc0; asm volatile("s_getpc_b64 %0" : "=s"(pc0));
    asm volatile("" :: "s"(xp), "s"(ckv), "s"(wq), "s"(wuk), "s"(wuv), "s"(wo), "s"(w1), "s"(b1), "s"(w2), "s"(b2), "s"(gw), "s"(out), "s"(wsp));
    const int bid = blockIdx.x, tid = threadIdx.x, lane = tid & 63;
    const int wave = __builtin_amdgcn_readfirstlane(tid >> 6);
    const int xcd = bid & 7, slot = bid >> 3;
    const int g = lane >> 4, n = lane & 15, q = (lane >> 2) & 3, p = lane & 3;
    const rsrc_t ws = __builtin_amdgcn_make_buffer_rsrc(wsp, 0, WS_END, 0x00020000);
    const v4i wsd = mk_srd(wsp, WS_END);
    const v4u CLEAN = {SENT, SENT, SENT, SENT};

    if (__builtin_expect(slot < 16, 1)) {
        const int a = xcd * 16 + slot, h = a >> 2, j = a & 3;
        asm volatile("s_nop 4" ::: "memory");
        v2u e = ald_e(wsd);
        if (wave == 0) st128(ws, CLEAN, lane < 32 ? WS_H1 + a * 512 + lane * 16 : WS_QA + (g & 1) * 32768 + h * 1024 + j * 256 + n * 16);
        {
            const v4i srd = mk_srd(wq + (size_t)j * 144 * 4096 + h * 128, 143 * 8192 + 256);
            const unsigned voff = g * 8192 + 16 * (n ^ ((4 * ((wave >> 1) & 1) + g) << 1));
#pragma unroll
            for (int i = 0; i < 5; ++i)
                if (i < 4 || wave < 4) dma16(srd, voff, (wave + 8 * i) * 32768, lds0 + LA_WQ + (wave + 8 * i) * 1024);
        }
        v4u xf[4]; v2u xt;
        {
            const v4i srd = mk_srd(xp, 2304);
            const unsigned xo = (n & 1) * 1152 + j * 288;
#pragma unroll
            for (int s = 0; s < 4; ++s) xf[s] = ald128p(srd, xo + 64 * s + 16 * g);
            xt = ald64p(srd, xo + 256 + 8 * g);
        }
        {
            const v4i srd = mk_srd(wuk + (size_t)j * 128 * 4096 + h * 128, 127 * 8192 + 256);
#pragma unroll
            for (int i = 0; i < 4; ++i)
                dma16(srd, g * 8192 + 16 * (n ^ (4 * i + g)), (16 * wave + 4 * i) * 8192, lds0 + LA_WUK + (16 * wave + 4 * i) * 256);
        }
        WAIT_VM(4, "+v"(e), "+v"(xf[0]), "+v"(xf[1]), "+v"(xf[2]), "+v"(xf[3]), "+v"(xt));
        const v2u tag = mk_tag(e);
        STAMP(1);
        barrier_lds();
        if (tid == 0) st64(ws, tag, WS_P1 + a * 128);
        STAMP(14);
        {
            const unsigned cw = 2 * wave + (p >> 1);
            const unsigned a0 = LA_WQ + (8 * g + q) * 256 + 16 * (cw ^ ((q + 4 * (g & 1)) << 1)) + 8 * (p & 1);
            const unsigned at = LA_WQ + (128 + 4 * g + q) * 256 + 16 * (cw ^ ((q + 4 * (g >> 1)) << 1)) + 8 * (p & 1);
            f32x4 c = {0.f, 0.f, 0.f, 0.f};
#pragma unroll
            for (int s = 0; s < 4; ++s) c = mfma32(lds_tr2(L, a0 + s * 8192, a0 + s * 8192 + 1024), xf[s], c);
            c = mfma16(lds_tr(L, at), xt, c);
            if (n < 2) st64(ws, (v2u){nos(pk(c[0], c[1])), nos(pk(c[2], c[3]))}, WS_H1 + a * 512 + wave * 64 + n * 32 + g * 8);
        }
        st128(ws, CLEAN, tid < 72 ? WS_M1 + a * 1152 + tid * 16 : tid < 360 ? WS_H5 + bid * 4608 + (tid - 72) * 16 : 0x7FFFFF00);
        STAMP(2);
        wait_flags<0, true>(ws, WS_P1 + (4 * h + p) * 128, tag);
        if (H1_DELAY) __builtin_amdgcn_s_sleep(H1_DELAY);
        STAMP(3);
        {
            v4u uf[4], qf[4]; unsigned off[4];
#pragma unroll
            for (int s = 0; s < 4; ++s) off[s] = WS_H1 + (4 * h + ((n >> 1) & 3)) * 512 + (2 * s + (g >> 1)) * 64 + (n & 1) * 32 + (g & 1) * 16;
            gather<4, H1_SLP>(ws, off, qf);
            if (tid == 0) st64(ws, (v2u){tag.x | 1u, tag.y}, WS_P1 + a * 128);
            STAMP(4);
#pragma unroll
            for (int s = 0; s < 4; ++s) uf[s] = lds128(L, LA_WUK + (16 * wave + n) * 256 + 16 * ((4 * s + g) ^ n));
            f32x4 c = {0.f, 0.f, 0.f, 0.f};
#pragma unroll
            for (int s = 0; s < 4; ++s) c = mfma32(uf[s], qf[s], c);
            constexpr float SC = 0.08838834764831845f * 1.4426950408889634f;
#pragma unroll
            for (int r = 0; r < 4; ++r) { c[r] += dpp<XOR2>(c[r]); c[r] += dpp<ROR4>(c[r]); c[r] *= SC; }
            if (n < 2) st64(ws, (v2u){nos(pk(c[0], c[1])), nos(pk(c[2], c[3]))}, WS_QA + n * 32768 + h * 1024 + (128 * j + 16 * wave + 4 * g) * 2);
        }
        STAMP(5);
        const int cs = j >> 1, ds = j & 1;
        if (A_DELAY) __builtin_amdgcn_s_sleep(A_DELAY);
        {
            const v4i srd = mk_srd(w1 + (size_t)j * 144 * 4608 + h * 144, 143 * 9216 + 288);
            const unsigned r3 = lane / 18, voff = r3 * 9216 + (lane - 18 * r3) * 16;
            if (lane < 54) {
#pragma unroll
                for (int i = 0; i < 6; ++i) dma16(srd, voff, (wave + 8 * i) * 27648, lds0 + LA_W1 + (wave + 8 * i) * 864);
            }
        }
        const unsigned sub_c = 16 * ((lane & 7) ^ ((((lane >> 4) & 1) | ((wave & 1) << 1)) << 1));
        {
            const v4i srd = mk_srd(wuv + (size_t)cs * 256 * 4096 + h * 128 + ds * 64, 255 * 8192 + 128);
#pragma unroll
            for (int i = 0; i < 4; ++i) dma16(srd, (lane >> 3) * 8192 + sub_c, (wave + 8 * i) * 65536, lds0 + LA_WUV + (wave + 8 * i) * 1024);
        }
        STAMP(6);
        wait_vm<4>();
        barrier_lds();
        if (tid == 0) st64(ws, tag, WS_F2 + a * 128);
        STAMP(7);
        {
            const unsigned a0 = LA_W1 + (8 * g + q) * 288 + 8 * p + 32 * wave, at = LA_W1 + (128 + 4 * g + q) * 288 + 8 * p + 32 * wave;
#pragma unroll
            for (int t = 0; t < 2; ++t) {
                if (t == 0 || wave == 0) {
                    f32x4 c = {0.f, 0.f, 0.f, 0.f};
#pragma unroll
                    for (int s = 0; s < 4; ++s) c = mfma32(lds_tr2(L, a0 + t * 256 + s * 9216, a0 + t * 256 + s * 9216 + 1152), xf[s], c);
                    c = mfma16(lds_tr(L, at + t * 256), xt, c);
                    const v4u v = {nos(__float_as_uint(c[0])), nos(__float_as_uint(c[1])), nos(__float_as_uint(c[2])), nos(__float_as_uint(c[3]))};
                    if (n < 2) st128(ws, v, WS_M1 + a * 1152 + n * 576 + (16 * wave + 128 * t + 4 * g) * 4);
                }
            }
        }
        STAMP(8);
        const int b = wave >> 2, hf = lane >> 5, li = lane & 31, k0 = 32 * b + 8 * (wave & 3) + 4 * hf;
        v4u r[4]; v2u ml[4]; unsigned off[4];
#pragma unroll
        for (int i = 0; i < 4; ++i) off[i] = WS_H3 + (k0 + i) * 32768 + h * 1024 + cs * 512 + li * 16;
        const unsigned offm = WS_ML + k0 * 256 + h * 8;
        {
            const v4i srd = mk_srd(wo + (size_t)(h * 128 + ds * 64) * 576, 64 * 1152);
#pragma unroll
            for (int i = 0; i < WO_BEFORE; ++i) dma16(srd, (lane >> 3) * 1152 + sub_c, wave * 9216 + i * 128, lds0 + LA_WO + wave * 9216 + i * 1024);
            if (wave == 0) wait_flags<0>(ws, WS_R3 + ((h >> 4) * 64 + lane) * 128, tag);
            barrier_lds();
            STAMP(9);
#pragma unroll
            for (int i = 0; i < 4; ++i) { r[i] = ald128(wsd, off[i]); ml[i] = ald64(wsd, offm + i * 256); }
#pragma unroll
            for (int i = WO_BEFORE; i < WO_MID; ++i) dma16(srd, (lane >> 3) * 1152 + sub_c, wave * 9216 + i * 128, lds0 + LA_WO + wave * 9216 + i * 1024);
            asm volatile("s_waitcnt vmcnt(%8)" : "+v"(r[0]), "+v"(r[1]), "+v"(r[2]), "+v"(r[3]), "+v"(ml[0]), "+v"(ml[1]), "+v"(ml[2]), "+v"(ml[3]) : "i"(WO_MID - WO_BEFORE) : "memory");
            STAMP(13);
#pragma unroll 1
            for (unsigned spins = 0; spins < SPIN_CAP; ++spins) {
                unsigned m = umax2(umax2(umax4(r[0]), umax4(r[1])), umax2(umax4(r[2]), umax4(r[3])));
                m = umax2(m, umax2(umax4((v4u){ml[0].x, ml[0].y, ml[1].x, ml[1].y}), umax4((v4u){ml[2].x, ml[2].y, ml[3].x, ml[3].y})));
                if (!__any(m == SENT)) break;
                asm volatile("" ::: "memory");
#pragma unroll
                for (int i = 0; i < 4; ++i) { r[i] = ld128(ws, off[i]); ml[i] = ld64(ws, offm + i * 256); }
            }
            STAMP(10);
            const float mx = fmaxf(fmaxf(__uint_as_float(ml[0].x), __uint_as_float(ml[1].x)), fmaxf(__uint_as_float(ml[2].x), __uint_as_float(ml[3].x)));
            float wk[4];
#pragma unroll
            for (int i = 0; i < 4; ++i) wk[i] = __builtin_amdgcn_exp2f(__uint_as_float(ml[i].x) - mx);
            float acc[8];
#pragma unroll
            for (int k = 0; k < 4; ++k) { acc[2 * k] = wk[0] * bflo(r[0][k]); acc[2 * k + 1] = wk[0] * bfhi(r[0][k]); }
#pragma unroll
            for (int i = 1; i < 4; ++i)
#pragma unroll
                for (int k = 0; k < 4; ++k) { acc[2 * k] = fmaf(wk[i], bflo(r[i][k]), acc[2 * k]); acc[2 * k + 1] = fmaf(wk[i], bfhi(r[i][k]), acc[2 * k + 1]); }
            const int col = 4 * (wave & 3) + 2 * hf + b;
            *(LAS v4u*)(L + LA_CMB + col * 512 + 16 * (li ^ col)) = (v4u){pk(acc[0], acc[1]), pk(acc[2], acc[3]), pk(acc[4], acc[5]), pk(acc[6], acc[7])};
            const float ls = (wk[0] * __uint_as_float(ml[0].y) + wk[1] * __uint_as_float(ml[1].y)) + (wk[2] * __uint_as_float(ml[2].y) + wk[3] * __uint_as_float(ml[3].y));
            if (li == 0) *(LAS f32x2*)(L + LA_COL + 8 * col) = (f32x2){mx, ls};
#pragma unroll
            for (int i = WO_MID; i < 9; ++i) dma16(srd, (lane >> 3) * 1152 + sub_c, wave * 9216 + i * 128, lds0 + LA_WO + wave * 9216 + i * 1024);
        }
        barrier_lds();
        if (wave < 4) {
            const unsigned a0 = LA_WUV + g * 1024 + q * 128 + 16 * ((2 * wave + (p >> 1)) ^ (((q >> 1) | ((g & 1) << 1)) << 1)) + 8 * (p & 1);
            f32x4 c = {0.f, 0.f, 0.f, 0.f};
#pragma unroll
            for (int s = 0; s < 8; ++s) c = mfma32(lds_tr2(L, a0 + s * 4096, a0 + s * 4096 + 512), lds128(L, LA_CMB + n * 512 + 16 * ((4 * s + g) ^ n)), c);
            const f32x2 cm = *(LAS f32x2*)(L + LA_COL + 8 * n);
            float mx = fmaxf(cm[0], dpp<XOR2>(cm[0])); mx = fmaxf(mx, dpp<ROR4>(mx)); mx = fmaxf(mx, dpp<ROR8>(mx));
            const float wc = __builtin_amdgcn_exp2f(cm[0] - mx);
            float ls = wc * cm[1]; ls += dpp<XOR2>(ls); ls += dpp<ROR4>(ls); ls += dpp<ROR8>(ls);
            const float sc = wc / ls;
#pragma unroll
            for (int r = 0; r < 4; ++r) { c[r] *= sc; c[r] += dpp<XOR2>(c[r]); c[r] += dpp<ROR4>(c[r]); c[r] += dpp<ROR8>(c[r]); }
            if (n < 2) *(LAS v2u*)(L + LA_CTX + n * 128 + (16 * wave + 4 * g) * 2) = (v2u){pk(c[0], c[1]), pk(c[2], c[3])};
        }
        wait_vm<0>();
        barrier_lds();
        STAMP(11);
        {
            v4u cf[2], of[5][2]; v2u ot[5];
#pragma unroll
            for (int s = 0; s < 2; ++s) cf[s] = lds128(L, LA_CTX + (n & 1) * 128 + 64 * s + 16 * g);
            proj576_load<2, false>(L, LA_WO, wave, lane, of, ot);
            proj576_run<2, false>(ws, bid, wave, lane, of, ot, cf, (v2u){0u, 0u});
        }
        STAMP(12);
        if (bid < 36) {
            float gate = 0.f, bias = 0.f;
            const int o = bid * 32 + (lane & 31), m = o - (bid / 18) * 576;
            if (wave == 0) {
                const GAS u16* xr = (const GAS u16*)xp + (bid / 18) * 576;
                float s = 0.f;
#pragma unroll
                for (int i = 0; i < 9; ++i) s += bfr(bflo(xr[lane + 64 * i]) * bflo(((const GAS u16*)gw)[lane + 64 * i]));
                s += dpp<XOR1>(s); s += dpp<XOR2>(s); s += dpp<ROR4>(s); s += dpp<ROR8>(s); s = x32sum(x16sum(s));
                gate = bfr(1.f / (1.f + __expf(-bfr(s))));
                bias = bflo(((const GAS u16*)b2)[m]);
            }
            if (wave < 4) wait_flags<2>(ws, WS_F2 + (wave * 64 + lane) * 128, tag);
            wait_vm<0>();
            barrier_lds();
            v4u r5[4]; unsigned o5[4];
#pragma unroll
            for (int i = 0; i < 4; ++i) o5[i] = WS_H5 + ((lane >> 3) + 8 * wave + 64 * i) * 4608 + bid * 128 + (lane & 7) * 16;
            gather<4>(ws, o5, r5);
            float t[4];
#pragma unroll
            for (int k = 0; k < 4; ++k) {
                const float sa = __uint_as_float(r5[0][k]) + __uint_as_float(r5[1][k]), sb = __uint_as_float(r5[2][k]) + __uint_as_float(r5[3][k]);
                auto s = __builtin_amdgcn_permlane32_swap(__float_as_uint(sa), __float_as_uint(sb), false, false);
                t[k] = __uint_as_float(s[0]) + __uint_as_float(s[1]);
                t[k] = x16sum(t[k]);
                t[k] += dpp<ROR8>(t[k]);
            }
            if ((lane & 24) == 0) *(LAS f32x4*)(L + LA_CMB + wave * 256 + (lane >> 5) * 128 + (lane & 7) * 16) = (f32x4){t[0], t[1], t[2], t[3]};
            barrier_lds();
            if (wave == 0) {
                float s = 0.f;
#pragma unroll
                for (int w = 0; w < 8; ++w) s += *(LAS float*)(L + LA_CMB + w * 256 + lane * 4);
                auto sw = __builtin_amdgcn_permlane32_swap(__float_as_uint(s), __float_as_uint(s), false, false);
                const float av = bfr(__uint_as_float(sw[0]));
                const float bv = bfr(bfr(__uint_as_float(sw[1])) + bias);
                const float res = bfr(gate * av) + bfr(bfr(1.f - gate) * bv);
                if (lane < 32) ((GAS u16*)out)[o] = (u16)pk(res, res);
                if (bid == 0 && lane == 0) st64(ws, tag, WS_E);
            }
            STAMP(15);
        }
    } else {
        const int sb = slot - 16, chunk = xcd * 8 + (sb >> 1), hh = sb & 1, b = chunk >> 5, bi = hh * 64 + chunk;
        asm volatile("s_nop 4" ::: "memory");
        unsigned pfv = 0;
        if (CODE_PF && tid < 120) {
            asm volatile("global_load_dword %0, %1, %2" : "=v"(pfv) : "v"(tid * 128), "s"(pc0 & ~127ull) : "memory");
        }
        v2u e = ald_e(wsd);
        if (B_DELAY) __builtin_amdgcn_s_sleep(B_DELAY);
        STAMP(1);
        v2u fa = {0u, 0u};
        {
            const v4i srd = mk_srd(ckv + (size_t)chunk * 128 * 512, 131072);
#pragma unroll
            for (int i = 0; i < 16; ++i) {
                if (i == FA_AT && wave == 0) fa = ald64(wsd, WS_P1 + (hh * 64 + lane) * 128);
                const int r = (i + 2 * wave + 8 * hh) & 15;
                dma16(srd, (16 * lane) ^ (16 * (2 * (r & 7) + (r >> 3))), (16 * wave + r) * 1024, lds0 + LB_CKV + (16 * wave + r) * 1024);
            }
        }
        STAMP(2);
        asm volatile("s_waitcnt vmcnt(%3)" : "+v"(fa), "+v"(e), "+v"(pfv) : "i"(16 - FA_AT) : "memory");
        const v2u tag = mk_tag(e);
        if (wave == 0 && !flags_ok2(fa, tag)) wait_flags<0>(ws, WS_P1 + (hh * 64 + lane) * 128, (v2u){tag.x | 1u, tag.y});
        barrier_lds();
        if (Q_DELAY) __builtin_amdgcn_s_sleep(Q_DELAY);
        STAMP(3);
        const int fsw = 2 * (n & 7) + (n >> 3), pg = ((g & 1) << 1) | (g >> 1);
        v4u kf[16];
        {
            v4u r[2]; unsigned off[2];
#pragma unroll
            for (int i = 0; i < 2; ++i) { off[i] = WS_QA + b * 32768 + hh * 16384 + i * 8192 + tid * 16; r[i] = ald128(wsd, off[i]); }
            {
                const unsigned o3 = WS_H3 + chunk * 32768 + hh * 16384 + tid * 16;
                st128(ws, CLEAN, o3); st128(ws, CLEAN, o3 + 8192);
                st128(ws, CLEAN, tid < 288 ? WS_H5 + bid * 4608 + tid * 16 : tid < 296 ? WS_ML + chunk * 256 + hh * 128 + (tid - 288) * 16 : 0x7FFFFF00);
            }
            wait_vm<5>();
            STAMP(12);
#pragma unroll
            for (int s = 0; s < 16; ++s) kf[s] = lds128(L, LB_CKV + (16 * wave + n) * 1024 + 16 * ((4 * (s & 3) + pg) ^ fsw) + 256 * (s >> 2));
            WAIT_VM(3, "+v"(r[0]), "+v"(r[1]));
            STAMP(13);
            if (!landed<2>(r)) gather<2, Q_SLP>(ws, off, r);
#pragma unroll
            for (int i = 0; i < 2; ++i) *(LAS v4u*)(L + LB_Q + (wave + 8 * i) * 1024 + 16 * (lane ^ (2 * wave + i))) = r[i];
        }
        STAMP(4);
        barrier_lds();
        STAMP(5);
        f32x4 sc = {0.f, 0.f, 0.f, 0.f};
#pragma unroll
        for (int s = 0; s < 16; ++s) sc = mfma32(kf[s], lds128(L, LB_Q + n * 1024 + 16 * ((4 * (s & 3) + pg) ^ fsw) + 256 * (s >> 2)), sc);
        v4u vf[4][4];
        {
            const int rr = 4 * g + q, fr = 2 * (rr & 7) + (rr >> 3);
#pragma unroll
            for (int t = 0; t < 4; ++t) {
                const unsigned a0 = LB_CKV + rr * 1024 + 256 * (wave >> 1) + 16 * ((8 * (wave & 1) + 2 * t + (p >> 1)) ^ fr) + 8 * (p & 1);
#pragma unroll
                for (int s = 0; s < 4; ++s) vf[t][s] = lds_tr2(L, a0 + s * 32768, a0 + s * 32768 + 16384);
            }
        }
        float mw = x32max(x16max(fmaxf(fmaxf(sc[0], sc[1]), fmaxf(sc[2], sc[3]))));
        if (g == 0) *(LAS float*)(L + LB_MX + n * 32 + wave * 4) = mw;
        barrier_lds();
        STAMP(6);
        float mx;
        {
            const f32x4 m0 = *(LAS f32x4*)(L + LB_MX + n * 32), m1 = *(LAS f32x4*)(L + LB_MX + n * 32 + 16);
            mx = fmaxf(fmaxf(fmaxf(m0[0], m0[1]), fmaxf(m0[2], m0[3])), fmaxf(fmaxf(m1[0], m1[1]), fmaxf(m1[2], m1[3])));
            const float p0 = __builtin_amdgcn_exp2f(sc[0] - mx), p1 = __builtin_amdgcn_exp2f(sc[1] - mx), p2 = __builtin_amdgcn_exp2f(sc[2] - mx), p3 = __builtin_amdgcn_exp2f(sc[3] - mx);
            *(LAS v2u*)(L + LB_P + (wave >> 1) * 1024 + lane * 16 + (wave & 1) * 8) = (v2u){pk(p0, p1), pk(p2, p3)};
            const float lw = x32sum(x16sum((p0 + p1) + (p2 + p3)));
            if (g == 0) *(LAS float*)(L + LB_L + n * 32 + wave * 4) = lw;
        }
        wait_vm<0>();
        barrier_lds();
        if (tid == 0) st64(ws, tag, WS_R3 + bi * 128);
        STAMP(7);
        {
            v4u pf[4];
#pragma unroll
            for (int s = 0; s < 4; ++s) pf[s] = lds128(L, LB_P + s * 1024 + lane * 16);
            if (wave == 7) {
                const f32x4 l0 = *(LAS f32x4*)(L + LB_L + n * 32), l1 = *(LAS f32x4*)(L + LB_L + n * 32 + 16);
                const float ls = ((l0[0] + l0[1]) + (l0[2] + l0[3])) + ((l1[0] + l1[1]) + (l1[2] + l1[3]));
                if (g == 0) st64(ws, (v2u){nos(__float_as_uint(mx)), nos(__float_as_uint(ls))}, WS_ML + chunk * 256 + (hh * 16 + n) * 8);
            }
            const unsigned o3 = WS_H3 + chunk * 32768 + (hh * 16 + n) * 1024 + wave * 128 + pg * 16;
#pragma unroll
            for (int u = 0; u < 2; ++u) {
                f32x4 c0 = {0.f, 0.f, 0.f, 0.f}, c1 = {0.f, 0.f, 0.f, 0.f};
#pragma unroll
                for (int s = 0; s < 4; ++s) { c0 = mfma32(vf[2 * u][s], pf[s], c0); c1 = mfma32(vf[2 * u + 1][s], pf[s], c1); }
                auto s0 = __builtin_amdgcn_permlane16_swap(pk(c0[0], c0[1]), pk(c1[0], c1[1]), false, false);
                auto s1 = __builtin_amdgcn_permlane16_swap(pk(c0[2], c0[3]), pk(c1[2], c1[3]), false, false);
                st128(ws, (v4u){nos(s0[0]), nos(s1[0]), nos(s0[1]), nos(s1[1])}, o3 + u * 64);
            }
        }
        STAMP(8);
        if (wave < 5) {
            const v4i srd = mk_srd(w2 + (size_t)bi * 36 * 576, 36 * 1152);
            const unsigned voff = (lane >> 3) * 1152 + 16 * ((lane & 7) ^ ((((lane >> 4) & 1) | ((wave & 1) << 1)) << 1));
#pragma unroll
            for (int i = 0; i < 9; ++i) dma16(srd, voff, wave * 9216 + i * 128, lds0 + LB_W2 + wave * 9216 + i * 1024);
        } else if (wave >= 6) {
            const int bb = wave - 6, f = lane < 36 ? lane : 35;
            const unsigned o1 = WS_M1 + (bi & ~3) * 1152 + bb * 576 + ((bi & 3) * 36 + f) * 4;
            const float bias = bflo(((const GAS u16*)b1)[bi * 36 + f]);
            unsigned r0, r1, r2, r3;
#pragma unroll 1
            for (unsigned spins = 0;; ++spins) {
                asm volatile("" ::: "memory");
                r0 = ld32(ws, o1); r1 = ld32(ws, o1 + 1152); r2 = ld32(ws, o1 + 2304); r3 = ld32(ws, o1 + 3456);
                if (!__any(umax2(umax2(r0, r1), umax2(r2, r3)) == SENT) || spins >= SPIN_CAP) break;
                __builtin_amdgcn_s_sleep(4);
            }
            const float v = bfr(bfr((__uint_as_float(r0) + __uint_as_float(r1)) + (__uint_as_float(r2) + __uint_as_float(r3))) + bias);
            const float u = 0.7978845608028654f * (v + 0.044715f * v * v * v);
            const float hv = v / (1.f + __expf(-2.f * u));
            if (lane < 48) *(LAS u16*)(L + LB_H + bb * 128 + lane * 2) = lane < 36 ? (u16)pk(hv, hv) : (u16)0;
        }
        STAMP(9);
        wait_vm<0>();
        barrier_lds();
        STAMP(10);
        {
            v4u wf[5][1], hf[1]; v2u wt[5];
            proj576_load<1, true>(L, LB_W2, wave, lane, wf, wt);
            hf[0] = lds128(L, LB_H + (n & 1) * 128 + 16 * g);
            const v2u ht = *(LAS v2u*)(L + LB_H + (n & 1) * 128 + 64 + 8 * g);
            proj576_run<1, true>(ws, bid, wave, lane, wf, wt, hf, ht);
        }
        STAMP(11);
    }
}

extern "C" void kernel_launch(void* const* d_in, const int* in_sizes, int n_in, void* d_out, int out_size, void* d_ws, size_t ws_size, hipStream_t stream) {
    fused_decode_kernel<<<256, NT, 0, stream>>>((const u16*)d_in[0], (const u16*)d_in[1], (const u16*)d_in[2], (const u16*)d_in[3], (const u16*)d_in[4],
                                                (const u16*)d_in[5], (const u16*)d_in[7], (const u16*)d_in[8], (const u16*)d_in[9], (const u16*)d_in[10],
                                                (const u16*)d_in[11], (u16*)d_out, (char*)d_ws);
}
```

```cpp
#define DBG_STAMP
#include <hip/hip_runtime.h>
#include <hip/hip_bf16.h>
#include <stdint.h>

typedef unsigned short u16;
typedef unsigned long long u64;
typedef unsigned v4u __attribute__((ext_vector_type(4)));
typedef unsigned v2u __attribute__((ext_vector_type(2)));
typedef int v4i __attribute__((ext_vector_type(4)));
typedef float f32x4 __attribute__((ext_vector_type(4)));
typedef float f32x2 __attribute__((ext_vector_type(2)));
typedef short s16x4 __attribute__((ext_vector_type(4)));
typedef __bf16 bf16x8 __attribute__((ext_vector_type(8)));
typedef __bf16 bf16x2 __attribute__((ext_vector_type(2)));
#define GAS __attribute__((address_space(1)))
#define LAS __attribute__((address_space(3)))
#define DI __device__ __forceinline__
typedef __amdgpu_buffer_rsrc_t rsrc_t;

constexpr int NT = 512;
constexpr unsigned SENT = 0xFFFFFFFFu;
constexpr int SC1 = 16;
constexpr unsigned SPIN_CAP = 1u << 17;
constexpr u64 TAG_STEP = 0x9E3779B97F4A7C16ull;

constexpr int WS_E = 0;
constexpr int WS_P1 = 4096;
constexpr int WS_F2 = WS_P1 + 16384;
constexpr int WS_R3 = WS_F2 + 16384;
constexpr int WS_H1 = WS_R3 + 16384;
constexpr int WS_QA = WS_H1 + 128 * 512;
constexpr int WS_M1 = WS_QA + 65536;
constexpr int WS_ML = WS_M1 + 128 * 1152;
constexpr int WS_H3 = WS_ML + 16384;
constexpr int WS_H5 = WS_H3 + 64 * 32768;
constexpr int WS_END = WS_H5 + 256 * 4608;
constexpr int WS_DBG = 8 << 20;

constexpr int LA_WQ = 0, LA_WUK = 36864, LA_WO = 0, LA_W1 = 73728, LA_WUV = 115200, LA_CMB = 147968, LA_CTX = 156160, LA_COL = 156416;
constexpr int LB_CKV = 0, LB_W2 = 0, LB_Q = 131072, LB_P = 147456, LB_MX = 151552, LB_L = 152064, LB_H = 152576;
constexpr int LDS_BYTES = 156672;

#define STAMP(i) do { __builtin_amdgcn_sched_barrier(0); asm volatile("s_memrealtime %0\n\ts_waitcnt lgkmcnt(0)" : "=s"(stamp[i]) :: "memory"); __builtin_amdgcn_sched_barrier(0); } while (0)

DI v4i mk_srd(const void* p, unsigned bytes) {
    const u64 a = (u64)p;
    return (v4i){(int)(unsigned)a, (int)((unsigned)(a >> 32) & 0xFFFFu), (int)bytes, 0x00020000};
}
DI void dma16(v4i srd, unsigned voff, unsigned soff, unsigned lds) {
    asm volatile("s_mov_b32 m0, %0\n\ts_nop 0\n\tbuffer_load_dwordx4 %1, %2, %3 offen lds" :: "s"(lds), "v"(voff), "s"(srd), "s"(soff) : "memory");
}
DI v4u ald128(v4i srd, unsigned voff) { v4u r; asm volatile("buffer_load_dwordx4 %0, %1, %2, 0 offen sc1" : "=v"(r) : "v"(voff), "s"(srd) : "memory"); return r; }
DI v4u ald128p(v4i srd, unsigned voff) { v4u r; asm volatile("buffer_load_dwordx4 %0, %1, %2, 0 offen" : "=v"(r) : "v"(voff), "s"(srd) : "memory"); return r; }
DI v2u ald64p(v4i srd, unsigned voff) { v2u r; asm volatile("buffer_load_dwordx2 %0, %1, %2, 0 offen" : "=v"(r) : "v"(voff), "s"(srd) : "memory"); return r; }
DI v2u ald_e(v4i srd) { v2u r; asm volatile("buffer_load_dwordx2 %0, off, %1, 0 sc1" : "=v"(r) : "s"(srd) : "memory"); return r; }
#define WAIT_VM(N, ...) asm volatile("s_waitcnt vmcnt(" #N ")" : __VA_ARGS__ :: "memory")
DI v2u ald64(v4i srd, unsigned voff) { v2u r; asm volatile("buffer_load_dwordx2 %0, %1, %2, 0 offen sc1" : "=v"(r) : "v"(voff), "s"(srd) : "memory"); return r; }
DI bool flags_ok(v2u f, v2u tag) { return __all((f.x == tag.x) & (f.y == tag.y)); }
DI bool flags_ok2(v2u f, v2u tag) { return __all(((f.x | 1u) == (tag.x | 1u)) & (f.y == tag.y)); }
DI v2u mk_tag(v2u e) { const u64 t = (((u64)e.y << 32) | e.x) + TAG_STEP; return (v2u){(unsigned)t & ~1u, (unsigned)(t >> 32)}; }
template <int N> DI void wait_vm() { asm volatile("s_waitcnt vmcnt(%0)" :: "i"(N) : "memory"); }
DI void barrier_lds() { asm volatile("s_waitcnt lgkmcnt(0)\n\ts_barrier" ::: "memory"); }
DI f32x4 mfma32(v4u a, v4u b, f32x4 c) { return __builtin_amdgcn_mfma_f32_16x16x32_bf16(__builtin_bit_cast(bf16x8, a), __builtin_bit_cast(bf16x8, b), c, 0, 0, 0); }
DI f32x4 mfma16(v2u a, v2u b, f32x4 c) { return mfma32((v4u){a.x, a.y, 0u, 0u}, (v4u){b.x, b.y, 0u, 0u}, c); }
DI v2u lds_tr(LAS char* L, unsigned off) { return __builtin_bit_cast(v2u, __builtin_amdgcn_ds_read_tr16_b64_v4i16((LAS s16x4*)(L + off))); }
DI v4u lds_tr2(LAS char* L, unsigned o0, unsigned o1) { const v2u a = lds_tr(L, o0), b = lds_tr(L, o1); return (v4u){a.x, a.y, b.x, b.y}; }
DI v4u lds128(LAS char* L, unsigned off) { return *(LAS v4u*)(L + off); }
DI unsigned pk(float lo, float hi) { return __builtin_bit_cast(unsigned, __builtin_convertvector((f32x2){lo, hi}, bf16x2)); }
DI unsigned nos(unsigned v) { return v < 0xFFFFFFFEu ? v : 0xFFFFFFFEu; }
DI float bflo(unsigned v) { return __uint_as_float(v << 16); }
DI float bfhi(unsigned v) { return __uint_as_float(v & 0xFFFF0000u); }
DI float bfr(float f) { return bflo(pk(f, f)); }
template <int CTRL> DI float dpp(float x) { return __builtin_bit_cast(float, __builtin_amdgcn_mov_dpp(__builtin_bit_cast(int, x), CTRL, 0xf, 0xf, true)); }
constexpr int XOR1 = 0xB1, XOR2 = 0x4E, ROR4 = 0x124, ROR8 = 0x128;
DI float x16max(float x) { auto s = __builtin_amdgcn_permlane16_swap(__float_as_uint(x), __float_as_uint(x), false, false); return fmaxf(__uint_as_float(s[0]), __uint_as_float(s[1])); }
DI float x32max(float x) { auto s = __builtin_amdgcn_permlane32_swap(__float_as_uint(x), __float_as_uint(x), false, false); return fmaxf(__uint_as_float(s[0]), __uint_as_float(s[1])); }
DI float x16sum(float x) { auto s = __builtin_amdgcn_permlane16_swap(__float_as_uint(x), __float_as_uint(x), false, false); return __uint_as_float(s[0]) + __uint_as_float(s[1]); }
DI float x32sum(float x) { auto s = __builtin_amdgcn_permlane32_swap(__float_as_uint(x), __float_as_uint(x), false, false); return __uint_as_float(s[0]) + __uint_as_float(s[1]); }
DI unsigned umax2(unsigned a, unsigned b) { return a > b ? a : b; }
DI unsigned umax4(v4u v) { return umax2(umax2(v.x, v.y), umax2(v.z, v.w)); }
DI void st128(rsrc_t rs, v4u v, unsigned off) { __builtin_amdgcn_raw_buffer_store_b128(v, rs, off, 0, SC1); }
DI void st64(rsrc_t rs, v2u v, unsigned off) { __builtin_amdgcn_raw_buffer_store_b64(v, rs, off, 0, SC1); }
DI v4u ld128(rsrc_t rs, unsigned off) { return __builtin_amdgcn_raw_buffer_load_b128(rs, off, 0, SC1); }
DI v2u ld64(rsrc_t rs, unsigned off) { return __builtin_amdgcn_raw_buffer_load_b64(rs, off, 0, SC1); }
DI unsigned ld32(rsrc_t rs, unsigned off) { return __builtin_amdgcn_raw_buffer_load_b32(rs, off, 0, SC1); }

template <int SLP, bool ANY = false> DI void wait_flags(rsrc_t rs, unsigned off, v2u tag) {
#pragma unroll 1
    for (unsigned spins = 0; spins < SPIN_CAP; ++spins) {
        asm volatile("" ::: "memory");
        const v2u f = ld64(rs, off);
        if (ANY ? flags_ok2(f, tag) : flags_ok(f, tag)) break;
        if (SLP) __builtin_amdgcn_s_sleep(SLP);
    }
    asm volatile("" ::: "memory");
}
template <int N> DI bool landed(const v4u (&r)[N]) {
    unsigned m = 0;
#pragma unroll
    for (int i = 0; i < N; ++i) m = umax2(m, umax4(r[i]));
    return !__any(m == SENT);
}
template <int N, int SLP = 0> DI void gather(rsrc_t rs, const unsigned (&off)[N], v4u (&r)[N]) {
#pragma unroll 1
    for (unsigned spins = 0;; ++spins) {
        asm volatile("" ::: "memory");
#pragma unroll
        for (int i = 0; i < N; ++i) r[i] = ld128(rs, off[i]);
        unsigned m = 0;
#pragma unroll
        for (int i = 0; i < N; ++i) m = umax2(m, umax4(r[i]));
        if (!__any(m == SENT) || spins >= SPIN_CAP) break;
        if (SLP) __builtin_amdgcn_s_sleep(SLP);
    }
}

template <int NS32, bool TAIL16>
DI void proj576_load(LAS char* L, int base, int wave, int lane, v4u (&wf)[5][NS32], v2u (&wt)[5]) {
    const int g = lane >> 4, q = (lane >> 2) & 3, p = lane & 3, tt = wave & 3, g0 = wave >> 2;
    const int x = (q >> 1) | ((g & 1) << 1);
    const unsigned a0 = base + g * 9216 + g0 * 1024 + q * 128 + 16 * ((2 * tt + (p >> 1)) ^ (x << 1)) + 8 * (p & 1);
    const unsigned at = base + 4 * NS32 * 9216 + g0 * 1024 + q * 128 + 16 * ((2 * tt + (p >> 1)) ^ ((q >> 1) << 1)) + 8 * (p & 1);
#pragma unroll
    for (int i = 0; i < 5; ++i) {
        if (i < 4 || g0 == 0) {
#pragma unroll
            for (int s = 0; s < NS32; ++s) wf[i][s] = lds_tr2(L, a0 + s * 36864 + i * 2048, a0 + s * 36864 + i * 2048 + 512);
            if (TAIL16) wt[i] = lds_tr(L, at + i * 2048);
        }
    }
}
template <int NS32, bool TAIL16>
DI void proj576_run(rsrc_t ws, int bid, int wave, int lane, const v4u (&wf)[5][NS32], const v2u (&wt)[5], const v4u (&bf)[NS32], v2u bt) {
    const int g = lane >> 4, n = lane & 15, tt = wave & 3, g0 = wave >> 2;
    const unsigned o0 = WS_H5 + bid * 4608 + (n & 1) * 2304 + (g0 * 64 + tt * 16 + 4 * g) * 4;
#pragma unroll
    for (int i = 0; i < 5; ++i) {
        if (i < 4 || g0 == 0) {
            f32x4 c = {0.f, 0.f, 0.f, 0.f};
#pragma unroll
            for (int s = 0; s < NS32; ++s) c = mfma32(wf[i][s], bf[s], c);
            if (TAIL16) c = mfma16(wt[i], bt, c);
            const v4u v = {nos(__float_as_uint(c[0])), nos(__float_as_uint(c[1])), nos(__float_as_uint(c[2])), nos(__float_as_uint(c[3]))};
            if (n < 2) st128(ws, v, o0 + i * 512);
        }
    }
}

#define WO_BEFORE 4
#define WO_MID 9
#define A_DELAY 0
#define H1_SLP 2
#define Q_SLP 4
#define FA_AT 10
#define B_DELAY 16
#define Q_DELAY 0
#define H1_DELAY 0
#define CODE_PF 1
__global__ void __launch_bounds__(NT, 2)
fused_decode_kernel(const u16* __restrict__ xp, const u16* __restrict__ ckv, const u16* __restrict__ wq, const u16* __restrict__ wuk,
                    const u16* __restrict__ wuv, const u16* __restrict__ wo, const u16* __restrict__ w1, const u16* __restrict__ b1,
                    const u16* __restrict__ w2, const u16* __restrict__ b2, const u16* __restrict__ gw, u16* __restrict__ out, char* wsp) {
    __shared__ __attribute__((aligned(1024))) char smem[LDS_BYTES];
    LAS char* L = (LAS char*)smem;
    const unsigned lds0 = (unsigned)(size_t)L;
    u64 stamp[16];
#pragma unroll
    for (int i = 0; i < 16; ++i) stamp[i] = 0;
    STAMP(0);
    u64 pc0; asm volatile("s_getpc_b64 %0" : "=s"(pc0));
    asm volatile("" :: "s"(xp), "s"(ckv), "s"(wq), "s"(wuk), "s"(wuv), "s"(wo), "s"(w1), "s"(b1), "s"(w2), "s"(b2), "s"(gw), "s"(out), "s"(wsp));
    const int bid = blockIdx.x, tid = threadIdx.x, lane = tid & 63;
    const int wave = __builtin_amdgcn_readfirstlane(tid >> 6);
    const int xcd = bid & 7, slot = bid >> 3;
    const int g = lane >> 4, n = lane & 15, q = (lane >> 2) & 3, p = lane & 3;
    const rsrc_t ws = __builtin_amdgcn_make_buffer_rsrc(wsp, 0, WS_END, 0x00020000);
    const v4i wsd = mk_srd(wsp, WS_END);
    const v4u CLEAN = {SENT, SENT, SENT, SENT};

    if (__builtin_expect(slot < 16, 1)) {
        const int a = xcd * 16 + slot, h = a >> 2, j = a & 3;
        asm volatile("s_nop 4" ::: "memory");
        v2u e = ald_e(wsd);
        if (wave == 0) st128(ws, CLEAN, lane < 32 ? WS_H1 + a * 512 + lane * 16 : WS_QA + (g & 1) * 32768 + h * 1024 + j * 256 + n * 16);
        {
            const v4i srd = mk_srd(wq + (size_t)j * 144 * 4096 + h * 128, 143 * 8192 + 256);
            const unsigned voff = g * 8192 + 16 * (n ^ ((4 * ((wave >> 1) & 1) + g) << 1));
#pragma unroll
            for (int i = 0; i < 5; ++i)
                if (i < 4 || wave < 4) dma16(srd, voff, (wave + 8 * i) * 32768, lds0 + LA_WQ + (wave + 8 * i) * 1024);
        }
        v4u xf[4]; v2u xt;
        {
            const v4i srd = mk_srd(xp, 2304);
            const unsigned xo = (n & 1) * 1152 + j * 288;
#pragma unroll
            for (int s = 0; s < 4; ++s) xf[s] = ald128p(srd, xo + 64 * s + 16 * g);
            xt = ald64p(srd, xo + 256 + 8 * g);
        }
        {
            const v4i srd = mk_srd(wuk + (size_t)j * 128 * 4096 + h * 128, 127 * 8192 + 256);
#pragma unroll
            for (int i = 0; i < 4; ++i)
                dma16(srd, g * 8192 + 16 * (n ^ (4 * i + g)), (16 * wave + 4 * i) * 8192, lds0 + LA_WUK + (16 * wave + 4 * i) * 256);
        }
        WAIT_VM(4, "+v"(e), "+v"(xf[0]), "+v"(xf[1]), "+v"(xf[2]), "+v"(xf[3]), "+v"(xt));
        const v2u tag = mk_tag(e);
        STAMP(1);
        barrier_lds();
        if (tid == 0) st64(ws, tag, WS_P1 + a * 128);
        STAMP(14);
        {
            const unsigned cw = 2 * wave + (p >> 1);
            const unsigned a0 = LA_WQ + (8 * g + q) * 256 + 16 * (cw ^ ((q + 4 * (g & 1)) << 1)) + 8 * (p & 1);
            const unsigned at = LA_WQ + (128 + 4 * g + q) * 256 + 16 * (cw ^ ((q + 4 * (g >> 1)) << 1)) + 8 * (p & 1);
            f32x4 c = {0.f, 0.f, 0.f, 0.f};
#pragma unroll
            for (int s = 0; s < 4; ++s) c = mfma32(lds_tr2(L, a0 + s * 8192, a0 + s * 8192 + 1024), xf[s], c);
            c = mfma16(lds_tr(L, at), xt, c);
            if (n < 2) st64(ws, (v2u){nos(pk(c[0], c[1])), nos(pk(c[2], c[3]))}, WS_H1 + a * 512 + wave * 64 + n * 32 + g * 8);
        }
        st128(ws, CLEAN, tid < 72 ? WS_M1 + a * 1152 + tid * 16 : tid < 360 ? WS_H5 + bid * 4608 + (tid - 72) * 16 : 0x7FFFFF00);
        STAMP(2);
        wait_flags<0, true>(ws, WS_P1 + (4 * h + p) * 128, tag);
        if (H1_DELAY) __builtin_amdgcn_s_sleep(H1_DELAY);
        STAMP(3);
        {
            v4u uf[4], qf[4]; unsigned off[4];
#pragma unroll
            for (int s = 0; s < 4; ++s) off[s] = WS_H1 + (4 * h + ((n >> 1) & 3)) * 512 + (2 * s + (g >> 1)) * 64 + (n & 1) * 32 + (g & 1) * 16;
            gather<4, H1_SLP>(ws, off, qf);
            if (tid == 0) st64(ws, (v2u){tag.x | 1u, tag.y}, WS_P1 + a * 128);
            STAMP(4);
#pragma unroll
            for (int s = 0; s < 4; ++s) uf[s] = lds128(L, LA_WUK + (16 * wave + n) * 256 + 16 * ((4 * s + g) ^ n));
            f32x4 c = {0.f, 0.f, 0.f, 0.f};
#pragma unroll
            for (int s = 0; s < 4; ++s) c = mfma32(uf[s], qf[s], c);
            constexpr float SC = 0.08838834764831845f * 1.4426950408889634f;
#pragma unroll
            for (int r = 0; r < 4; ++r) { c[r] += dpp<XOR2>(c[r]); c[r] += dpp<ROR4>(c[r]); c[r] *= SC; }
            if (n < 2) st64(ws, (v2u){nos(pk(c[0], c[1])), nos(pk(c[2], c[3]))}, WS_QA + n * 32768 + h * 1024 + (128 * j + 16 * wave + 4 * g) * 2);
        }
        STAMP(5);
        const int cs = j >> 1, ds = j & 1;
        if (A_DELAY) __builtin_amdgcn_s_sleep(A_DELAY);
        {
            const v4i srd = mk_srd(w1 + (size_t)j * 144 * 4608 + h * 144, 143 * 9216 + 288);
            const unsigned r3 = lane / 18, voff = r3 * 9216 + (lane - 18 * r3) * 16;
            if (lane < 54) {
#pragma unroll
                for (int i = 0; i < 6; ++i) dma16(srd, voff, (wave + 8 * i) * 27648, lds0 + LA_W1 + (wave + 8 * i) * 864);
            }
        }
        const unsigned sub_c = 16 * ((lane & 7) ^ ((((lane >> 4) & 1) | ((wave & 1) << 1)) << 1));
        {
            const v4i srd = mk_srd(wuv + (size_t)cs * 256 * 4096 + h * 128 + ds * 64, 255 * 8192 + 128);
#pragma unroll
            for (int i = 0; i < 4; ++i) dma16(srd, (lane >> 3) * 8192 + sub_c, (wave + 8 * i) * 65536, lds0 + LA_WUV + (wave + 8 * i) * 1024);
        }
        STAMP(6);
        wait_vm<4>();
        barrier_lds();
        if (tid == 0) st64(ws, tag, WS_F2 + a * 128);
        STAMP(7);
        {
            const unsigned a0 = LA_W1 + (8 * g + q) * 288 + 8 * p + 32 * wave, at = LA_W1 + (128 + 4 * g + q) * 288 + 8 * p + 32 * wave;
#pragma unroll
            for (int t = 0; t < 2; ++t) {
                if (t == 0 || wave == 0) {
                    f32x4 c = {0.f, 0.f, 0.f, 0.f};
#pragma unroll
                    for (int s = 0; s < 4; ++s) c = mfma32(lds_tr2(L, a0 + t * 256 + s * 9216, a0 + t * 256 + s * 9216 + 1152), xf[s], c);
                    c = mfma16(lds_tr(L, at + t * 256), xt, c);
                    const v4u v = {nos(__float_as_uint(c[0])), nos(__float_as_uint(c[1])), nos(__float_as_uint(c[2])), nos(__float_as_uint(c[3]))};
                    if (n < 2) st128(ws, v, WS_M1 + a * 1152 + n * 576 + (16 * wave + 128 * t + 4 * g) * 4);
                }
            }
        }
        STAMP(8);
        const int b = wave >> 2, hf = lane >> 5, li = lane & 31, k0 = 32 * b + 8 * (wave & 3) + 4 * hf;
        v4u r[4]; v2u ml[4]; unsigned off[4];
#pragma unroll
        for (int i = 0; i < 4; ++i) off[i] = WS_H3 + (k0 + i) * 32768 + h * 1024 + cs * 512 + li * 16;
        const unsigned offm = WS_ML + k0 * 256 + h * 8;
        {
            const v4i srd = mk_srd(wo + (size_t)(h * 128 + ds * 64) * 576, 64 * 1152);
#pragma unroll
            for (int i = 0; i < WO_BEFORE; ++i) dma16(srd, (lane >> 3) * 1152 + sub_c, wave * 9216 + i * 128, lds0 + LA_WO + wave * 9216 + i * 1024);
            if (wave == 0) wait_flags<0>(ws, WS_R3 + ((h >> 4) * 64 + lane) * 128, tag);
            barrier_lds();
            STAMP(9);
#pragma unroll
            for (int i = 0; i < 4; ++i) { r[i] = ald128(wsd, off[i]); ml[i] = ald64(wsd, offm + i * 256); }
#pragma unroll
            for (int i = WO_BEFORE; i < WO_MID; ++i) dma16(srd, (lane >> 3) * 1152 + sub_c, wave * 9216 + i * 128, lds0 + LA_WO + wave * 9216 + i * 1024);
            asm volatile("s_waitcnt vmcnt(%8)" : "+v"(r[0]), "+v"(r[1]), "+v"(r[2]), "+v"(r[3]), "+v"(ml[0]), "+v"(ml[1]), "+v"(ml[2]), "+v"(ml[3]) : "i"(WO_MID - WO_BEFORE) : "memory");
            STAMP(13);
#pragma unroll 1
            for (unsigned spins = 0; spins < SPIN_CAP; ++spins) {
                unsigned m = umax2(umax2(umax4(r[0]), umax4(r[1])), umax2(umax4(r[2]), umax4(r[3])));
                m = umax2(m, umax2(umax4((v4u){ml[0].x, ml[0].y, ml[1].x, ml[1].y}), umax4((v4u){ml[2].x, ml[2].y, ml[3].x, ml[3].y})));
                if (!__any(m == SENT)) break;
                asm volatile("" ::: "memory");
#pragma unroll
                for (int i = 0; i < 4; ++i) { r[i] = ld128(ws, off[i]); ml[i] = ld64(ws, offm + i * 256); }
            }
            STAMP(10);
            const float mx = fmaxf(fmaxf(__uint_as_float(ml[0].x), __uint_as_float(ml[1].x)), fmaxf(__uint_as_float(ml[2].x), __uint_as_float(ml[3].x)));
            float wk[4];
#pragma unroll
            for (int i = 0; i < 4; ++i) wk[i] = __builtin_amdgcn_exp2f(__uint_as_float(ml[i].x) - mx);
            float acc[8];
#pragma unroll
            for (int k = 0; k < 4; ++k) { acc[2 * k] = wk[0] * bflo(r[0][k]); acc[2 * k + 1] = wk[0] * bfhi(r[0][k]); }
#pragma unroll
            for (int i = 1; i < 4; ++i)
#pragma unroll
                for (int k = 0; k < 4; ++k) { acc[2 * k] = fmaf(wk[i], bflo(r[i][k]), acc[2 * k]); acc[2 * k + 1] = fmaf(wk[i], bfhi(r[i][k]), acc[2 * k + 1]); }
            const int col = 4 * (wave & 3) + 2 * hf + b;
            *(LAS v4u*)(L + LA_CMB + col * 512 + 16 * (li ^ col)) = (v4u){pk(acc[0], acc[1]), pk(acc[2], acc[3]), pk(acc[4], acc[5]), pk(acc[6], acc[7])};
            const float ls = (wk[0] * __uint_as_float(ml[0].y) + wk[1] * __uint_as_float(ml[1].y)) + (wk[2] * __uint_as_float(ml[2].y) + wk[3] * __uint_as_float(ml[3].y));
            if (li == 0) *(LAS f32x2*)(L + LA_COL + 8 * col) = (f32x2){mx, ls};
#pragma unroll
            for (int i = WO_MID; i < 9; ++i) dma16(srd, (lane >> 3) * 1152 + sub_c, wave * 9216 + i * 128, lds0 + LA_WO + wave * 9216 + i * 1024);
        }
        barrier_lds();
        if (wave < 4) {
            const unsigned a0 = LA_WUV + g * 1024 + q * 128 + 16 * ((2 * wave + (p >> 1)) ^ (((q >> 1) | ((g & 1) << 1)) << 1)) + 8 * (p & 1);
            f32x4 c = {0.f, 0.f, 0.f, 0.f};
#pragma unroll
            for (int s = 0; s < 8; ++s) c = mfma32(lds_tr2(L, a0 + s * 4096, a0 + s * 4096 + 512), lds128(L, LA_CMB + n * 512 + 16 * ((4 * s + g) ^ n)), c);
            const f32x2 cm = *(LAS f32x2*)(L + LA_COL + 8 * n);
            float mx = fmaxf(cm[0], dpp<XOR2>(cm[0])); mx = fmaxf(mx, dpp<ROR4>(mx)); mx = fmaxf(mx, dpp<ROR8>(mx));
            const float wc = __builtin_amdgcn_exp2f(cm[0] - mx);
            float ls = wc * cm[1]; ls += dpp<XOR2>(ls); ls += dpp<ROR4>(ls); ls += dpp<ROR8>(ls);
            const float sc = wc / ls;
#pragma unroll
            for (int r = 0; r < 4; ++r) { c[r] *= sc; c[r] += dpp<XOR2>(c[r]); c[r] += dpp<ROR4>(c[r]); c[r] += dpp<ROR8>(c[r]); }
            if (n < 2) *(LAS v2u*)(L + LA_CTX + n * 128 + (16 * wave + 4 * g) * 2) = (v2u){pk(c[0], c[1]), pk(c[2], c[3])};
        }
        wait_vm<0>();
        barrier_lds();
        STAMP(11);
        {
            v4u cf[2], of[5][2]; v2u ot[5];
#pragma unroll
            for (int s = 0; s < 2; ++s) cf[s] = lds128(L, LA_CTX + (n & 1) * 128 + 64 * s + 16 * g);
            proj576_load<2, false>(L, LA_WO, wave, lane, of, ot);
            proj576_run<2, false>(ws, bid, wave, lane, of, ot, cf, (v2u){0u, 0u});
        }
        STAMP(12);
        if (bid < 36) {
            float gate = 0.f, bias = 0.f;
            const int o = bid * 32 + (lane & 31), m = o - (bid / 18) * 576;
            if (wave == 0) {
                const GAS u16* xr = (const GAS u16*)xp + (bid / 18) * 576;
                float s = 0.f;
#pragma unroll
                for (int i = 0; i < 9; ++i) s += bfr(bflo(xr[lane + 64 * i]) * bflo(((const GAS u16*)gw)[lane + 64 * i]));
                s += dpp<XOR1>(s); s += dpp<XOR2>(s); s += dpp<ROR4>(s); s += dpp<ROR8>(s); s = x32sum(x16sum(s));
                gate = bfr(1.f / (1.f + __expf(-bfr(s))));
                bias = bflo(((const GAS u16*)b2)[m]);
            }
            if (wave < 4) wait_flags<2>(ws, WS_F2 + (wave * 64 + lane) * 128, tag);
            wait_vm<0>();
            barrier_lds();
            v4u r5[4]; unsigned o5[4];
#pragma unroll
            for (int i = 0; i < 4; ++i) o5[i] = WS_H5 + ((lane >> 3) + 8 * wave + 64 * i) * 4608 + bid * 128 + (lane & 7) * 16;
            gather<4>(ws, o5, r5);
            float t[4];
#pragma unroll
            for (int k = 0; k < 4; ++k) {
                const float sa = __uint_as_float(r5[0][k]) + __uint_as_float(r5[1][k]), sb = __uint_as_float(r5[2][k]) + __uint_as_float(r5[3][k]);
                auto s = __builtin_amdgcn_permlane32_swap(__float_as_uint(sa), __float_as_uint(sb), false, false);
                t[k] = __uint_as_float(s[0]) + __uint_as_float(s[1]);
                t[k] = x16sum(t[k]);
                t[k] += dpp<ROR8>(t[k]);
            }
            if ((lane & 24) == 0) *(LAS f32x4*)(L + LA_CMB + wave * 256 + (lane >> 5) * 128 + (lane & 7) * 16) = (f32x4){t[0], t[1], t[2], t[3]};
            barrier_lds();
            if (wave == 0) {
                float s = 0.f;
#pragma unroll
                for (int w = 0; w < 8; ++w) s += *(LAS float*)(L + LA_CMB + w * 256 + lane * 4);
                auto sw = __builtin_amdgcn_permlane32_swap(__float_as_uint(s), __float_as_uint(s), false, false);
                const float av = bfr(__uint_as_float(sw[0]));
                const float bv = bfr(bfr(__uint_as_float(sw[1])) + bias);
                const float res = bfr(gate * av) + bfr(bfr(1.f - gate) * bv);
                if (lane < 32) ((GAS u16*)out)[o] = (u16)pk(res, res);
                if (bid == 0 && lane == 0) st64(ws, tag, WS_E);
            }
            STAMP(15);
        }
    } else {
        const int sb = slot - 16, chunk = xcd * 8 + (sb >> 1), hh = sb & 1, b = chunk >> 5, bi = hh * 64 + chunk;
        asm volatile("s_nop 4" ::: "memory");
        unsigned pfv = 0;
        if (CODE_PF && tid < 120) {
            asm volatile("global_load_dword %0, %1, %2" : "=v"(pfv) : "v"(tid * 128), "s"(pc0 & ~127ull) : "memory");
        }
        v2u e = ald_e(wsd);
        if (B_DELAY) __builtin_amdgcn_s_sleep(B_DELAY);
        STAMP(1);
        v2u fa = {0u, 0u};
        {
            const v4i srd = mk_srd(ckv + (size_t)chunk * 128 * 512, 131072);
#pragma unroll
            for (int i = 0; i < 16; ++i) {
                if (i == FA_AT && wave == 0) fa = ald64(wsd, WS_P1 + (hh * 64 + lane) * 128);
                const int r = (i + 2 * wave + 8 * hh) & 15;
                dma16(srd, (16 * lane) ^ (16 * (2 * (r & 7) + (r >> 3))), (16 * wave + r) * 1024, lds0 + LB_CKV + (16 * wave + r) * 1024);
            }
        }
        STAMP(2);
        asm volatile("s_waitcnt vmcnt(%3)" : "+v"(fa), "+v"(e), "+v"(pfv) : "i"(16 - FA_AT) : "memory");
        const v2u tag = mk_tag(e);
        if (wave == 0 && !flags_ok2(fa, tag)) wait_flags<0>(ws, WS_P1 + (hh * 64 + lane) * 128, (v2u){tag.x | 1u, tag.y});
        barrier_lds();
        if (Q_DELAY) __builtin_amdgcn_s_sleep(Q_DELAY);
        STAMP(3);
        const int fsw = 2 * (n & 7) + (n >> 3), pg = ((g & 1) << 1) | (g >> 1);
        v4u kf[16];
        {
            v4u r[2]; unsigned off[2];
#pragma unroll
            for (int i = 0; i < 2; ++i) { off[i] = WS_QA + b * 32768 + hh * 16384 + i * 8192 + tid * 16; r[i] = ald128(wsd, off[i]); }
            {
                const unsigned o3 = WS_H3 + chunk * 32768 + hh * 16384 + tid * 16;
                st128(ws, CLEAN, o3); st128(ws, CLEAN, o3 + 8192);
                st128(ws, CLEAN, tid < 288 ? WS_H5 + bid * 4608 + tid * 16 : tid < 296 ? WS_ML + chunk * 256 + hh * 128 + (tid - 288) * 16 : 0x7FFFFF00);
            }
            wait_vm<5>();
            STAMP(12);
#pragma unroll
            for (int s = 0; s < 16; ++s) kf[s] = lds128(L, LB_CKV + (16 * wave + n) * 1024 + 16 * ((4 * (s & 3) + pg) ^ fsw) + 256 * (s >> 2));
            WAIT_VM(3, "+v"(r[0]), "+v"(r[1]));
            STAMP(13);
            if (!landed<2>(r)) gather<2, Q_SLP>(ws, off, r);
#pragma unroll
            for (int i = 0; i < 2; ++i) *(LAS v4u*)(L + LB_Q + (wave + 8 * i) * 1024 + 16 * (lane ^ (2 * wave + i))) = r[i];
        }
        STAMP(4);
        barrier_lds();
        STAMP(5);
        f32x4 sc = {0.f, 0.f, 0.f, 0.f};
#pragma unroll
        for (int s = 0; s < 16; ++s) sc = mfma32(kf[s], lds128(L, LB_Q + n * 1024 + 16 * ((4 * (s & 3) + pg) ^ fsw) + 256 * (s >> 2)), sc);
        v4u vf[4][4];
        {
            const int rr = 4 * g + q, fr = 2 * (rr & 7) + (rr >> 3);
#pragma unroll
            for (int t = 0; t < 4; ++t) {
                const unsigned a0 = LB_CKV + rr * 1024 + 256 * (wave >> 1) + 16 * ((8 * (wave & 1) + 2 * t + (p >> 1)) ^ fr) + 8 * (p & 1);
#pragma unroll
                for (int s = 0; s < 4; ++s) vf[t][s] = lds_tr2(L, a0 + s * 32768, a0 + s * 32768 + 16384);
            }
        }
        float mw = x32max(x16max(fmaxf(fmaxf(sc[0], sc[1]), fmaxf(sc[2], sc[3]))));
        if (g == 0) *(LAS float*)(L + LB_MX + n * 32 + wave * 4) = mw;
        barrier_lds();
        STAMP(6);
        float mx;
        {
            const f32x4 m0 = *(LAS f32x4*)(L + LB_MX + n * 32), m1 = *(LAS f32x4*)(L + LB_MX + n * 32 + 16);
            mx = fmaxf(fmaxf(fmaxf(m0[0], m0[1]), fmaxf(m0[2], m0[3])), fmaxf(fmaxf(m1[0], m1[1]), fmaxf(m1[2], m1[3])));
            const float p0 = __builtin_amdgcn_exp2f(sc[0] - mx), p1 = __builtin_amdgcn_exp2f(sc[1] - mx), p2 = __builtin_amdgcn_exp2f(sc[2] - mx), p3 = __builtin_amdgcn_exp2f(sc[3] - mx);
            *(LAS v2u*)(L + LB_P + (wave >> 1) * 1024 + lane * 16 + (wave & 1) * 8) = (v2u){pk(p0, p1), pk(p2, p3)};
            const float lw = x32sum(x16sum((p0 + p1) + (p2 + p3)));
            if (g == 0) *(LAS float*)(L + LB_L + n * 32 + wave * 4) = lw;
        }
        wait_vm<0>();
        barrier_lds();
        if (tid == 0) st64(ws, tag, WS_R3 + bi * 128);
        STAMP(7);
        {
            v4u pf[4];
#pragma unroll
            for (int s = 0; s < 4; ++s) pf[s] = lds128(L, LB_P + s * 1024 + lane * 16);
            if (wave == 7) {
                const f32x4 l0 = *(LAS f32x4*)(L + LB_L + n * 32), l1 = *(LAS f32x4*)(L + LB_L + n * 32 + 16);
                const float ls = ((l0[0] + l0[1]) + (l0[2] + l0[3])) + ((l1[0] + l1[1]) + (l1[2] + l1[3]));
                if (g == 0) st64(ws, (v2u){nos(__float_as_uint(mx)), nos(__float_as_uint(ls))}, WS_ML + chunk * 256 + (hh * 16 + n) * 8);
            }
            const unsigned o3 = WS_H3 + chunk * 32768 + (hh * 16 + n) * 1024 + wave * 128 + pg * 16;
#pragma unroll
            for (int u = 0; u < 2; ++u) {
                f32x4 c0 = {0.f, 0.f, 0.f, 0.f}, c1 = {0.f, 0.f, 0.f, 0.f};
#pragma unroll
                for (int s = 0; s < 4; ++s) { c0 = mfma32(vf[2 * u][s], pf[s], c0); c1 = mfma32(vf[2 * u + 1][s], pf[s], c1); }
                auto s0 = __builtin_amdgcn_permlane16_swap(pk(c0[0], c0[1]), pk(c1[0], c1[1]), false, false);
                auto s1 = __builtin_amdgcn_permlane16_swap(pk(c0[2], c0[3]), pk(c1[2], c1[3]), false, false);
                st128(ws, (v4u){nos(s0[0]), nos(s1[0]), nos(s0[1]), nos(s1[1])}, o3 + u * 64);
            }
        }
        STAMP(8);
        if (wave < 5) {
            const v4i srd = mk_srd(w2 + (size_t)bi * 36 * 576, 36 * 1152);
            const unsigned voff = (lane >> 3) * 1152 + 16 * ((lane & 7) ^ ((((lane >> 4) & 1) | ((wave & 1) << 1)) << 1));
#pragma unroll
            for (int i = 0; i < 9; ++i) dma16(srd, voff, wave * 9216 + i * 128, lds0 + LB_W2 + wave * 9216 + i * 1024);
        } else if (wave >= 6) {
            const int bb = wave - 6, f = lane < 36 ? lane : 35;
            const unsigned o1 = WS_M1 + (bi & ~3) * 1152 + bb * 576 + ((bi & 3) * 36 + f) * 4;
            const float bias = bflo(((const GAS u16*)b1)[bi * 36 + f]);
            unsigned r0, r1, r2, r3;
#pragma unroll 1
            for (unsigned spins = 0;; ++spins) {
                asm volatile("" ::: "memory");
                r0 = ld32(ws, o1); r1 = ld32(ws, o1 + 1152); r2 = ld32(ws, o1 + 2304); r3 = ld32(ws, o1 + 3456);
                if (!__any(umax2(umax2(r0, r1), umax2(r2, r3)) == SENT) || spins >= SPIN_CAP) break;
                __builtin_amdgcn_s_sleep(4);
            }
            const float v = bfr(bfr((__uint_as_float(r0) + __uint_as_float(r1)) + (__uint_as_float(r2) + __uint_as_float(r3))) + bias);
            const float u = 0.7978845608028654f * (v + 0.044715f * v * v * v);
            const float hv = v / (1.f + __expf(-2.f * u));
            if (lane < 48) *(LAS u16*)(L + LB_H + bb * 128 + lane * 2) = lane < 36 ? (u16)pk(hv, hv) : (u16)0;
        }
        STAMP(9);
        wait_vm<0>();
        barrier_lds();
        STAMP(10);
        {
            v4u wf[5][1], hf[1]; v2u wt[5];
            proj576_load<1, true>(L, LB_W2, wave, lane, wf, wt);
            hf[0] = lds128(L, LB_H + (n & 1) * 128 + 16 * g);
            const v2u ht = *(LAS v2u*)(L + LB_H + (n & 1) * 128 + 64 + 8 * g);
            proj576_run<1, true>(ws, bid, wave, lane, wf, wt, hf, ht);
        }
        STAMP(11);
    }
    if (lane == 0) {
        GAS u64* d = (GAS u64*)(wsp + WS_DBG) + (bid * 8 + wave) * 16;
#pragma unroll
        for (int i = 0; i < 16; ++i) d[i] = stamp[i];
    }
}

template <int ROLE, int IDX, int MODE>
__global__ void spin_kernel(char* wsp) {
    __shared__ u64 sm[3][256];
    const GAS u64* d = (const GAS u64*)(wsp + WS_DBG);
    u64 t0min = ~0ull, t0max = 0, v = MODE == 1 ? ~0ull : 0;
    const int bid = threadIdx.x;
    for (int w = 0; w < 8; ++w) {
        const GAS u64* s = d + (bid * 8 + w) * 16;
        t0min = s[0] < t0min ? s[0] : t0min; t0max = s[0] > t0max ? s[0] : t0max;
        if (ROLE == 2 || ((bid >> 3) >= 16) == ROLE) {
            if (IDX >= 0) { u64 x = s[IDX]; if (x) { if (MODE == 1) v = x < v ? x : v; else v = x > v ? x : v; } }
            else for (int i = 0; i < 16; ++i) v = s[i] > v ? s[i] : v;
        }
    }
    sm[0][bid] = t0min; sm[1][bid] = t0max; sm[2][bid] = v;
    __syncthreads();
    if (bid == 0) {
        for (int i = 0; i < 256; ++i) { t0min = sm[0][i] < t0min ? sm[0][i] : t0min; t0max = sm[1][i] > t0max ? sm[1][i] : t0max; u64 x = sm[2][i]; if (MODE == 1) v = x < v ? x : v; else v = x > v ? x : v; }
        u64 span = MODE == 2 ? 0 : v - t0min;
        if (span > 10000) span = 10000;
        u64 t, t1; asm volatile("s_memrealtime %0\n\ts_waitcnt lgkmcnt(0)" : "=s"(t) :: "memory");
        do { asm volatile("s_memrealtime %0\n\ts_waitcnt lgkmcnt(0)" : "=s"(t1) :: "memory"); } while (t1 < t + span * 100);
    }
}
extern "C" void kernel_launch(void* const* d_in, const int* in_sizes, int n_in, void* d_out, int out_size, void* d_ws, size_t ws_size, hipStream_t stream) {
    fused_decode_kernel<<<256, NT, 0, stream>>>((const u16*)d_in[0], (const u16*)d_in[1], (const u16*)d_in[2], (const u16*)d_in[3], (const u16*)d_in[4],
                                                (const u16*)d_in[5], (const u16*)d_in[7], (const u16*)d_in[8], (const u16*)d_in[9], (const u16*)d_in[10],
                                                (const u16*)d_in[11], (u16*)d_out, (char*)d_ws);
    spin_kernel<2, 0, 2><<<1, 256, 0, stream>>>((char*)d_ws);
}
```

```cpp
#define DBG_STAMP
#include <hip/hip_runtime.h>
#include <hip/hip_bf16.h>
#include <stdint.h>

typedef unsigned short u16;
typedef unsigned long long u64;
typedef unsigned v4u __attribute__((ext_vector_type(4)));
typedef unsigned v2u __attribute__((ext_vector_type(2)));
typedef int v4i __attribute__((ext_vector_type(4)));
typedef float f32x4 __attribute__((ext_vector_type(4)));
typedef float f32x2 __attribute__((ext_vector_type(2)));
typedef short s16x4 __attribute__((ext_vector_type(4)));
typedef __bf16 bf16x8 __attribute__((ext_vector_type(8)));
typedef __bf16 bf16x2 __attribute__((ext_vector_type(2)));
#define GAS __attribute__((address_space(1)))
#define LAS __attribute__((address_space(3)))
#define DI __device__ __forceinline__
typedef __amdgpu_buffer_rsrc_t rsrc_t;

constexpr int NT = 512;
constexpr unsigned SENT = 0xFFFFFFFFu;
constexpr int SC1 = 16;
constexpr unsigned SPIN_CAP = 1u << 17;
constexpr u64 TAG_STEP = 0x9E3779B97F4A7C16ull;

constexpr int WS_E = 0;
constexpr int WS_P1 = 4096;
constexpr int WS_F2 = WS_P1 + 16384;
constexpr int WS_R3 = WS_F2 + 16384;
constexpr int WS_H1 = WS_R3 + 16384;
constexpr int WS_QA = WS_H1 + 128 * 512;
constexpr int WS_M1 = WS_QA + 65536;
constexpr int WS_ML = WS_M1 + 128 * 1152;
constexpr int WS_H3 = WS_ML + 16384;
constexpr int WS_H5 = WS_H3 + 64 * 32768;
constexpr int WS_END = WS_H5 + 256 * 4608;
constexpr int WS_DBG = 8 << 20;

constexpr int LA_WQ = 0, LA_WUK = 36864, LA_WO = 0, LA_W1 = 73728, LA_WUV = 115200, LA_CMB = 147968, LA_CTX = 156160, LA_COL = 156416;
constexpr int LB_CKV = 0, LB_W2 = 0, LB_Q = 131072, LB_P = 147456, LB_MX = 151552, LB_L = 152064, LB_H = 152576;
constexpr int LDS_BYTES = 156672;

#define STAMP(i) do { __builtin_amdgcn_sched_barrier(0); asm volatile("s_memrealtime %0\n\ts_waitcnt lgkmcnt(0)" : "=s"(stamp[i]) :: "memory"); __builtin_amdgcn_sched_barrier(0); } while (0)

DI v4i mk_srd(const void* p, unsigned bytes) {
    const u64 a = (u64)p;
    return (v4i){(int)(unsigned)a, (int)((unsigned)(a >> 32) & 0xFFFFu), (int)bytes, 0x00020000};
}
DI void dma16(v4i srd, unsigned voff, unsigned soff, unsigned lds) {
    asm volatile("s_mov_b32 m0, %0\n\ts_nop 0\n\tbuffer_load_dwordx4 %1, %2, %3 offen lds" :: "s"(lds), "v"(voff), "s"(srd), "s"(soff) : "memory");
}
DI v4u ald128(v4i srd, unsigned voff) { v4u r; asm volatile("buffer_load_dwordx4 %0, %1, %2, 0 offen sc1" : "=v"(r) : "v"(voff), "s"(srd) : "memory"); return r; }
DI v4u ald128p(v4i srd, unsigned voff) { v4u r; asm volatile("buffer_load_dwordx4 %0, %1, %2, 0 offen" : "=v"(r) : "v"(voff), "s"(srd) : "memory"); return r; }
DI v2u ald64p(v4i srd, unsigned voff) { v2u r; asm volatile("buffer_load_dwordx2 %0, %1, %2, 0 offen" : "=v"(r) : "v"(voff), "s"(srd) : "memory"); return r; }
DI v2u ald_e(v4i srd) { v2u r; asm volatile("buffer_load_dwordx2 %0, off, %1, 0 sc1" : "=v"(r) : "s"(srd) : "memory"); return r; }
#define WAIT_VM(N, ...) asm volatile("s_waitcnt vmcnt(" #N ")" : __VA_ARGS__ :: "memory")
DI v2u ald64(v4i srd, unsigned voff) { v2u r; asm volatile("buffer_load_dwordx2 %0, %1, %2, 0 offen sc1" : "=v"(r) : "v"(voff), "s"(srd) : "memory"); return r; }
DI bool flags_ok(v2u f, v2u tag) { return __all((f.x == tag.x) & (f.y == tag.y)); }
DI bool flags_ok2(v2u f, v2u tag) { return __all(((f.x | 1u) == (tag.x | 1u)) & (f.y == tag.y)); }
DI v2u mk_tag(v2u e) { const u64 t = (((u64)e.y << 32) | e.x) + TAG_STEP; return (v2u){(unsigned)t & ~1u, (unsigned)(t >> 32)}; }
template <int N> DI void wait_vm() { asm volatile("s_waitcnt vmcnt(%0)" :: "i"(N) : "memory"); }
DI void barrier_lds() { asm volatile("s_waitcnt lgkmcnt(0)\n\ts_barrier" ::: "memory"); }
DI f32x4 mfma32(v4u a, v4u b, f32x4 c) { return __builtin_amdgcn_mfma_f32_16x16x32_bf16(__builtin_bit_cast(bf16x8, a), __builtin_bit_cast(bf16x8, b), c, 0, 0, 0); }
DI f32x4 mfma16(v2u a, v2u b, f32x4 c) { return mfma32((v4u){a.x, a.y, 0u, 0u}, (v4u){b.x, b.y, 0u, 0u}, c); }
DI v2u lds_tr(LAS char* L, unsigned off) { return __builtin_bit_cast(v2u, __builtin_amdgcn_ds_read_tr16_b64_v4i16((LAS s16x4*)(L + off))); }
DI v4u lds_tr2(LAS char* L, unsigned o0, unsigned o1) { const v2u a = lds_tr(L, o0), b = lds_tr(L, o1); return (v4u){a.x, a.y, b.x, b.y}; }
DI v4u lds128(LAS char* L, unsigned off) { return *(LAS v4u*)(L + off); }
DI unsigned pk(float lo, float hi) { return __builtin_bit_cast(unsigned, __builtin_convertvector((f32x2){lo, hi}, bf16x2)); }
DI unsigned nos(unsigned v) { return v < 0xFFFFFFFEu ? v : 0xFFFFFFFEu; }
DI float bflo(unsigned v) { return __uint_as_float(v << 16); }
DI float bfhi(unsigned v) { return __uint_as_float(v & 0xFFFF0000u); }
DI float bfr(float f) { return bflo(pk(f, f)); }
template <int CTRL> DI float dpp(float x) { return __builtin_bit_cast(float, __builtin_amdgcn_mov_dpp(__builtin_bit_cast(int, x), CTRL, 0xf, 0xf, true)); }
constexpr int XOR1 = 0xB1, XOR2 = 0x4E, ROR4 = 0x124, ROR8 = 0x128;
DI float x16max(float x) { auto s = __builtin_amdgcn_permlane16_swap(__float_as_uint(x), __float_as_uint(x), false, false); return fmaxf(__uint_as_float(s[0]), __uint_as_float(s[1])); }
DI float x32max(float x) { auto s = __builtin_amdgcn_permlane32_swap(__float_as_uint(x), __float_as_uint(x), false, false); return fmaxf(__uint_as_float(s[0]), __uint_as_float(s[1])); }
DI float x16sum(float x) { auto s = __builtin_amdgcn_permlane16_swap(__float_as_uint(x), __float_as_uint(x), false, false); return __uint_as_float(s[0]) + __uint_as_float(s[1]); }
DI float x32sum(float x) { auto s = __builtin_amdgcn_permlane32_swap(__float_as_uint(x), __float_as_uint(x), false, false); return __uint_as_float(s[0]) + __uint_as_float(s[1]); }
DI unsigned umax2(unsigned a, unsigned b) { return a > b ? a : b; }
DI unsigned umax4(v4u v) { return umax2(umax2(v.x, v.y), umax2(v.z, v.w)); }
DI void st128(rsrc_t rs, v4u v, unsigned off) { __builtin_amdgcn_raw_buffer_store_b128(v, rs, off, 0, SC1); }
DI void st64(rsrc_t rs, v2u v, unsigned off) { __builtin_amdgcn_raw_buffer_store_b64(v, rs, off, 0, SC1); }
DI v4u ld128(rsrc_t rs, unsigned off) { return __builtin_amdgcn_raw_buffer_load_b128(rs, off, 0, SC1); }
DI v2u ld64(rsrc_t rs, unsigned off) { return __builtin_amdgcn_raw_buffer_load_b64(rs, off, 0, SC1); }
DI unsigned ld32(rsrc_t rs, unsigned off) { return __builtin_amdgcn_raw_buffer_load_b32(rs, off, 0, SC1); }

template <int SLP, bool ANY = false> DI void wait_flags(rsrc_t rs, unsigned off, v2u tag) {
#pragma unroll 1
    for (unsigned spins = 0; spins < SPIN_CAP; ++spins) {
        asm volatile("" ::: "memory");
        const v2u f = ld64(rs, off);
        if (ANY ? flags_ok2(f, tag) : flags_ok(f, tag)) break;
        if (SLP) __builtin_amdgcn_s_sleep(SLP);
    }
    asm volatile("" ::: "memory");
}
template <int N> DI bool landed(const v4u (&r)[N]) {
    unsigned m = 0;
#pragma unroll
    for (int i = 0; i < N; ++i) m = umax2(m, umax4(r[i]));
    return !__any(m == SENT);
}
template <int N, int SLP = 0> DI void gather(rsrc_t rs, const unsigned (&off)[N], v4u (&r)[N]) {
#pragma unroll 1
    for (unsigned spins = 0;; ++spins) {
        asm volatile("" ::: "memory");
#pragma unroll
        for (int i = 0; i < N; ++i) r[i] = ld128(rs, off[i]);
        unsigned m = 0;
#pragma unroll
        for (int i = 0; i < N; ++i) m = umax2(m, umax4(r[i]));
        if (!__any(m == SENT) || spins >= SPIN_CAP) break;
        if (SLP) __builtin_amdgcn_s_sleep(SLP);
    }
}

template <int NS32, bool TAIL16>
DI void proj576_load(LAS char* L, int base, int wave, int lane, v4u (&wf)[5][NS32], v2u (&wt)[5]) {
    const int g = lane >> 4, q = (lane >> 2) & 3, p = lane & 3, tt = wave & 3, g0 = wave >> 2;
    const int x = (q >> 1) | ((g & 1) << 1);
    const unsigned a0 = base + g * 9216 + g0 * 1024 + q * 128 + 16 * ((2 * tt + (p >> 1)) ^ (x << 1)) + 8 * (p & 1);
    const unsigned at = base + 4 * NS32 * 9216 + g0 * 1024 + q * 128 + 16 * ((2 * tt + (p >> 1)) ^ ((q >> 1) << 1)) + 8 * (p & 1);
#pragma unroll
    for (int i = 0; i < 5; ++i) {
        if (i < 4 || g0 == 0) {
#pragma unroll
            for (int s = 0; s < NS32; ++s) wf[i][s] = lds_tr2(L, a0 + s * 36864 + i * 2048, a0 + s * 36864 + i * 2048 + 512);
            if (TAIL16) wt[i] = lds_tr(L, at + i * 2048);
        }
    }
}
template <int NS32, bool TAIL16>
DI void proj576_run(rsrc_t ws, int bid, int wave, int lane, const v4u (&wf)[5][NS32], const v2u (&wt)[5], const v4u (&bf)[NS32], v2u bt) {
    const int g = lane >> 4, n = lane & 15, tt = wave & 3, g0 = wave >> 2;
    const unsigned o0 = WS_H5 + bid * 4608 + (n & 1) * 2304 + (g0 * 64 + tt * 16 + 4 * g) * 4;
#pragma unroll
    for (int i = 0; i < 5; ++i) {
        if (i < 4 || g0 == 0) {
            f32x4 c = {0.f, 0.f, 0.f, 0.f};
#pragma unroll
            for (int s = 0; s < NS32; ++s) c = mfma32(wf[i][s], bf[s], c);
            if (TAIL16) c = mfma16(wt[i], bt, c);
            const v4u v = {nos(__float_as_uint(c[0])), nos(__float_as_uint(c[1])), nos(__float_as_uint(c[2])), nos(__float_as_uint(c[3]))};
            if (n < 2) st128(ws, v, o0 + i * 512);
        }
    }
}

#define WO_BEFORE 4
#define WO_MID 9
#define A_DELAY 0
#define H1_SLP 2
#define Q_SLP 4
#define FA_AT 10
#define B_DELAY 16
#define Q_DELAY 0
#define H1_DELAY 0
#define CODE_PF 1
__global__ void __launch_bounds__(NT, 2)
fused_decode_kernel(const u16* __restrict__ xp, const u16* __restrict__ ckv, const u16* __restrict__ wq, const u16* __restrict__ wuk,
                    const u16* __restrict__ wuv, const u16* __restrict__ wo, const u16* __restrict__ w1, const u16* __restrict__ b1,
                    const u16* __restrict__ w2, const u16* __restrict__ b2, const u16* __restrict__ gw, u16* __restrict__ out, char* wsp) {
    __shared__ __attribute__((aligned(1024))) char smem[LDS_BYTES];
    LAS char* L = (LAS char*)smem;
    const unsigned lds0 = (unsigned)(size_t)L;
    u64 stamp[16];
#pragma unroll
    for (int i = 0; i < 16; ++i) stamp[i] = 0;
    STAMP(0);
    u64 pc0; asm volatile("s_getpc_b64 %0" : "=s"(pc0));
    asm volatile("" :: "s"(xp), "s"(ckv), "s"(wq), "s"(wuk), "s"(wuv), "s"(wo), "s"(w1), "s"(b1), "s"(w2), "s"(b2), "s"(gw), "s"(out), "s"(wsp));
    const int bid = blockIdx.x, tid = threadIdx.x, lane = tid & 63;
    const int wave = __builtin_amdgcn_readfirstlane(tid >> 6);
    const int xcd = bid & 7, slot = bid >> 3;
    const int g = lane >> 4, n = lane & 15, q = (lane >> 2) & 3, p = lane & 3;
    const rsrc_t ws = __builtin_amdgcn_make_buffer_rsrc(wsp, 0, WS_END, 0x00020000);
    const v4i wsd = mk_srd(wsp, WS_END);
    const v4u CLEAN = {SENT, SENT, SENT, SENT};

    if (__builtin_expect(slot < 16, 1)) {
        const int a = xcd * 16 + slot, h = a >> 2, j = a & 3;
        asm volatile("s_nop 4" ::: "memory");
        v2u e = ald_e(wsd);
        if (wave == 0) st128(ws, CLEAN, lane < 32 ? WS_H1 + a * 512 + lane * 16 : WS_QA + (g & 1) * 32768 + h * 1024 + j * 256 + n * 16);
        {
            const v4i srd = mk_srd(wq + (size_t)j * 144 * 4096 + h * 128, 143 * 8192 + 256);
            const unsigned voff = g * 8192 + 16 * (n ^ ((4 * ((wave >> 1) & 1) + g) << 1));
#pragma unroll
            for (int i = 0; i < 5; ++i)
                if (i < 4 || wave < 4) dma16(srd, voff, (wave + 8 * i) * 32768, lds0 + LA_WQ + (wave + 8 * i) * 1024);
        }
        v4u xf[4]; v2u xt;
        {
            const v4i srd = mk_srd(xp, 2304);
            const unsigned xo = (n & 1) * 1152 + j * 288;
#pragma unroll
            for (int s = 0; s < 4; ++s) xf[s] = ald128p(srd, xo + 64 * s + 16 * g);
            xt = ald64p(srd, xo + 256 + 8 * g);
        }
        {
            const v4i srd = mk_srd(wuk + (size_t)j * 128 * 4096 + h * 128, 127 * 8192 + 256);
#pragma unroll
            for (int i = 0; i < 4; ++i)
                dma16(srd, g * 8192 + 16 * (n ^ (4 * i + g)), (16 * wave + 4 * i) * 8192, lds0 + LA_WUK + (16 * wave + 4 * i) * 256);
        }
        WAIT_VM(4, "+v"(e), "+v"(xf[0]), "+v"(xf[1]), "+v"(xf[2]), "+v"(xf[3]), "+v"(xt));
        const v2u tag = mk_tag(e);
        STAMP(1);
        barrier_lds();
        if (tid == 0) st64(ws, tag, WS_P1 + a * 128);
        STAMP(14);
        {
            const unsigned cw = 2 * wave + (p >> 1);
            const unsigned a0 = LA_WQ + (8 * g + q) * 256 + 16 * (cw ^ ((q + 4 * (g & 1)) << 1)) + 8 * (p & 1);
            const unsigned at = LA_WQ + (128 + 4 * g + q) * 256 + 16 * (cw ^ ((q + 4 * (g >> 1)) << 1)) + 8 * (p & 1);
            f32x4 c = {0.f, 0.f, 0.f, 0.f};
#pragma unroll
            for (int s = 0; s < 4; ++s) c = mfma32(lds_tr2(L, a0 + s * 8192, a0 + s * 8192 + 1024), xf[s], c);
            c = mfma16(lds_tr(L, at), xt, c);
            if (n < 2) st64(ws, (v2u){nos(pk(c[0], c[1])), nos(pk(c[2], c[3]))}, WS_H1 + a * 512 + wave * 64 + n * 32 + g * 8);
        }
        st128(ws, CLEAN, tid < 72 ? WS_M1 + a * 1152 + tid * 16 : tid < 360 ? WS_H5 + bid * 4608 + (tid - 72) * 16 : 0x7FFFFF00);
        STAMP(2);
        wait_flags<0, true>(ws, WS_P1 + (4 * h + p) * 128, tag);
        if (H1_DELAY) __builtin_amdgcn_s_sleep(H1_DELAY);
        STAMP(3);
        {
            v4u uf[4], qf[4]; unsigned off[4];
#pragma unroll
            for (int s = 0; s < 4; ++s) off[s] = WS_H1 + (4 * h + ((n >> 1) & 3)) * 512 + (2 * s + (g >> 1)) * 64 + (n & 1) * 32 + (g & 1) * 16;
            gather<4, H1_SLP>(ws, off, qf);
            if (tid == 0) st64(ws, (v2u){tag.x | 1u, tag.y}, WS_P1 + a * 128);
            STAMP(4);
#pragma unroll
            for (int s = 0; s < 4; ++s) uf[s] = lds128(L, LA_WUK + (16 * wave + n) * 256 + 16 * ((4 * s + g) ^ n));
            f32x4 c = {0.f, 0.f, 0.f, 0.f};
#pragma unroll
            for (int s = 0; s < 4; ++s) c = mfma32(uf[s], qf[s], c);
            constexpr float SC = 0.08838834764831845f * 1.4426950408889634f;
#pragma unroll
            for (int r = 0; r < 4; ++r) { c[r] += dpp<XOR2>(c[r]); c[r] += dpp<ROR4>(c[r]); c[r] *= SC; }
            if (n < 2) st64(ws, (v2u){nos(pk(c[0], c[1])), nos(pk(c[2], c[3]))}, WS_QA + n * 32768 + h * 1024 + (128 * j + 16 * wave + 4 * g) * 2);
        }
        STAMP(5);
        const int cs = j >> 1, ds = j & 1;
        if (A_DELAY) __builtin_amdgcn_s_sleep(A_DELAY);
        {
            const v4i srd = mk_srd(w1 + (size_t)j * 144 * 4608 + h * 144, 143 * 9216 + 288);
            const unsigned r3 = lane / 18, voff = r3 * 9216 + (lane - 18 * r3) * 16;
            if (lane < 54) {
#pragma unroll
                for (int i = 0; i < 6; ++i) dma16(srd, voff, (wave + 8 * i) * 27648, lds0 + LA_W1 + (wave + 8 * i) * 864);
            }
        }
        const unsigned sub_c = 16 * ((lane & 7) ^ ((((lane >> 4) & 1) | ((wave & 1) << 1)) << 1));
        {
            const v4i srd = mk_srd(wuv + (size_t)cs * 256 * 4096 + h * 128 + ds * 64, 255 * 8192 + 128);
#pragma unroll
            for (int i = 0; i < 4; ++i) dma16(srd, (lane >> 3) * 8192 + sub_c, (wave + 8 * i) * 65536, lds0 + LA_WUV + (wave + 8 * i) * 1024);
        }
        STAMP(6);
        wait_vm<4>();
        barrier_lds();
        if (tid == 0) st64(ws, tag, WS_F2 + a * 128);
        STAMP(7);
        {
            const unsigned a0 = LA_W1 + (8 * g + q) * 288 + 8 * p + 32 * wave, at = LA_W1 + (128 + 4 * g + q) * 288 + 8 * p + 32 * wave;
#pragma unroll
            for (int t = 0; t < 2; ++t) {
                if (t == 0 || wave == 0) {
                    f32x4 c = {0.f, 0.f, 0.f, 0.f};
#pragma unroll
                    for (int s = 0; s < 4; ++s) c = mfma32(lds_tr2(L, a0 + t * 256 + s * 9216, a0 + t * 256 + s * 9216 + 1152), xf[s], c);
                    c = mfma16(lds_tr(L, at + t * 256), xt, c);
                    const v4u v = {nos(__float_as_uint(c[0])), nos(__float_as_uint(c[1])), nos(__float_as_uint(c[2])), nos(__float_as_uint(c[3]))};
                    if (n < 2) st128(ws, v, WS_M1 + a * 1152 + n * 576 + (16 * wave + 128 * t + 4 * g) * 4);
                }
            }
        }
        STAMP(8);
        const int b = wave >> 2, hf = lane >> 5, li = lane & 31, k0 = 32 * b + 8 * (wave & 3) + 4 * hf;
        v4u r[4]; v2u ml[4]; unsigned off[4];
#pragma unroll
        for (int i = 0; i < 4; ++i) off[i] = WS_H3 + (k0 + i) * 32768 + h * 1024 + cs * 512 + li * 16;
        const unsigned offm = WS_ML + k0 * 256 + h * 8;
        {
            const v4i srd = mk_srd(wo + (size_t)(h * 128 + ds * 64) * 576, 64 * 1152);
#pragma unroll
            for (int i = 0; i < WO_BEFORE; ++i) dma16(srd, (lane >> 3) * 1152 + sub_c, wave * 9216 + i * 128, lds0 + LA_WO + wave * 9216 + i * 1024);
            if (wave == 0) wait_flags<0>(ws, WS_R3 + ((h >> 4) * 64 + lane) * 128, tag);
            barrier_lds();
            STAMP(9);
#pragma unroll
            for (int i = 0; i < 4; ++i) { r[i] = ald128(wsd, off[i]); ml[i] = ald64(wsd, offm + i * 256); }
#pragma unroll
            for (int i = WO_BEFORE; i < WO_MID; ++i) dma16(srd, (lane >> 3) * 1152 + sub_c, wave * 9216 + i * 128, lds0 + LA_WO + wave * 9216 + i * 1024);
            asm volatile("s_waitcnt vmcnt(%8)" : "+v"(r[0]), "+v"(r[1]), "+v"(r[2]), "+v"(r[3]), "+v"(ml[0]), "+v"(ml[1]), "+v"(ml[2]), "+v"(ml[3]) : "i"(WO_MID - WO_BEFORE) : "memory");
            STAMP(13);
#pragma unroll 1
            for (unsigned spins = 0; spins < SPIN_CAP; ++spins) {
                unsigned m = umax2(umax2(umax4(r[0]), umax4(r[1])), umax2(umax4(r[2]), umax4(r[3])));
                m = umax2(m, umax2(umax4((v4u){ml[0].x, ml[0].y, ml[1].x, ml[1].y}), umax4((v4u){ml[2].x, ml[2].y, ml[3].x, ml[3].y})));
                if (!__any(m == SENT)) break;
                asm volatile("" ::: "memory");
#pragma unroll
                for (int i = 0; i < 4; ++i) { r[i] = ld128(ws, off[i]); ml[i] = ld64(ws, offm + i * 256); }
            }
            STAMP(10);
            const float mx = fmaxf(fmaxf(__uint_as_float(ml[0].x), __uint_as_float(ml[1].x)), fmaxf(__uint_as_float(ml[2].x), __uint_as_float(ml[3].x)));
            float wk[4];
#pragma unroll
            for (int i = 0; i < 4; ++i) wk[i] = __builtin_amdgcn_exp2f(__uint_as_float(ml[i].x) - mx);
            float acc[8];
#pragma unroll
            for (int k = 0; k < 4; ++k) { acc[2 * k] = wk[0] * bflo(r[0][k]); acc[2 * k + 1] = wk[0] * bfhi(r[0][k]); }
#pragma unroll
            for (int i = 1; i < 4; ++i)
#pragma unroll
                for (int k = 0; k < 4; ++k) { acc[2 * k] = fmaf(wk[i], bflo(r[i][k]), acc[2 * k]); acc[2 * k + 1] = fmaf(wk[i], bfhi(r[i][k]), acc[2 * k + 1]); }
            const int col = 4 * (wave & 3) + 2 * hf + b;
            *(LAS v4u*)(L + LA_CMB + col * 512 + 16 * (li ^ col)) = (v4u){pk(acc[0], acc[1]), pk(acc[2], acc[3]), pk(acc[4], acc[5]), pk(acc[6], acc[7])};
            const float ls = (wk[0] * __uint_as_float(ml[0].y) + wk[1] * __uint_as_float(ml[1].y)) + (wk[2] * __uint_as_float(ml[2].y) + wk[3] * __uint_as_float(ml[3].y));
            if (li == 0) *(LAS f32x2*)(L + LA_COL + 8 * col) = (f32x2){mx, ls};
#pragma unroll
            for (int i = WO_MID; i < 9; ++i) dma16(srd, (lane >> 3) * 1152 + sub_c, wave * 9216 + i * 128, lds0 + LA_WO + wave * 9216 + i * 1024);
        }
        barrier_lds();
        if (wave < 4) {
            const unsigned a0 = LA_WUV + g * 1024 + q * 128 + 16 * ((2 * wave + (p >> 1)) ^ (((q >> 1) | ((g & 1) << 1)) << 1)) + 8 * (p & 1);
            f32x4 c = {0.f, 0.f, 0.f, 0.f};
#pragma unroll
            for (int s = 0; s < 8; ++s) c = mfma32(lds_tr2(L, a0 + s * 4096, a0 + s * 4096 + 512), lds128(L, LA_CMB + n * 512 + 16 * ((4 * s + g) ^ n)), c);
            const f32x2 cm = *(LAS f32x2*)(L + LA_COL + 8 * n);
            float mx = fmaxf(cm[0], dpp<XOR2>(cm[0])); mx = fmaxf(mx, dpp<ROR4>(mx)); mx = fmaxf(mx, dpp<ROR8>(mx));
            const float wc = __builtin_amdgcn_exp2f(cm[0] - mx);
            float ls = wc * cm[1]; ls += dpp<XOR2>(ls); ls += dpp<ROR4>(ls); ls += dpp<ROR8>(ls);
            const float sc = wc / ls;
#pragma unroll
            for (int r = 0; r < 4; ++r) { c[r] *= sc; c[r] += dpp<XOR2>(c[r]); c[r] += dpp<ROR4>(c[r]); c[r] += dpp<ROR8>(c[r]); }
            if (n < 2) *(LAS v2u*)(L + LA_CTX + n * 128 + (16 * wave + 4 * g) * 2) = (v2u){pk(c[0], c[1]), pk(c[2], c[3])};
        }
        wait_vm<0>();
        barrier_lds();
        STAMP(11);
        {
            v4u cf[2], of[5][2]; v2u ot[5];
#pragma unroll
            for (int s = 0; s < 2; ++s) cf[s] = lds128(L, LA_CTX + (n & 1) * 128 + 64 * s + 16 * g);
            proj576_load<2, false>(L, LA_WO, wave, lane, of, ot);
            proj576_run<2, false>(ws, bid, wave, lane, of, ot, cf, (v2u){0u, 0u});
        }
        STAMP(12);
        if (bid < 36) {
            float gate = 0.f, bias = 0.f;
            const int o = bid * 32 + (lane & 31), m = o - (bid / 18) * 576;
            if (wave == 0) {
                const GAS u16* xr = (const GAS u16*)xp + (bid / 18) * 576;
                float s = 0.f;
#pragma unroll
                for (int i = 0; i < 9; ++i) s += bfr(bflo(xr[lane + 64 * i]) * bflo(((const GAS u16*)gw)[lane + 64 * i]));
                s += dpp<XOR1>(s); s += dpp<XOR2>(s); s += dpp<ROR4>(s); s += dpp<ROR8>(s); s = x32sum(x16sum(s));
                gate = bfr(1.f / (1.f + __expf(-bfr(s))));
                bias = bflo(((const GAS u16*)b2)[m]);
            }
            if (wave < 4) wait_flags<2>(ws, WS_F2 + (wave * 64 + lane) * 128, tag);
            wait_vm<0>();
            barrier_lds();
            v4u r5[4]; unsigned o5[4];
#pragma unroll
            for (int i = 0; i < 4; ++i) o5[i] = WS_H5 + ((lane >> 3) + 8 * wave + 64 * i) * 4608 + bid * 128 + (lane & 7) * 16;
            gather<4>(ws, o5, r5);
            float t[4];
#pragma unroll
            for (int k = 0; k < 4; ++k) {
                const float sa = __uint_as_float(r5[0][k]) + __uint_as_float(r5[1][k]), sb = __uint_as_float(r5[2][k]) + __uint_as_float(r5[3][k]);
                auto s = __builtin_amdgcn_permlane32_swap(__float_as_uint(sa), __float_as_uint(sb), false, false);
                t[k] = __uint_as_float(s[0]) + __uint_as_float(s[1]);
                t[k] = x16sum(t[k]);
                t[k] += dpp<ROR8>(t[k]);
            }
            if ((lane & 24) == 0) *(LAS f32x4*)(L + LA_CMB + wave * 256 + (lane >> 5) * 128 + (lane & 7) * 16) = (f32x4){t[0], t[1], t[2], t[3]};
            barrier_lds();
            if (wave == 0) {
                float s = 0.f;
#pragma unroll
                for (int w = 0; w < 8; ++w) s += *(LAS float*)(L + LA_CMB + w * 256 + lane * 4);
                auto sw = __builtin_amdgcn_permlane32_swap(__float_as_uint(s), __float_as_uint(s), false, false);
                const float av = bfr(__uint_as_float(sw[0]));
                const float bv = bfr(bfr(__uint_as_float(sw[1])) + bias);
                const float res = bfr(gate * av) + bfr(bfr(1.f - gate) * bv);
                if (lane < 32) ((GAS u16*)out)[o] = (u16)pk(res, res);
                if (bid == 0 && lane == 0) st64(ws, tag, WS_E);
            }
            STAMP(15);
        }
    } else {
        const int sb = slot - 16, chunk = xcd * 8 + (sb >> 1), hh = sb & 1, b = chunk >> 5, bi = hh * 64 + chunk;
        asm volatile("s_nop 4" ::: "memory");
        unsigned pfv = 0;
        if (CODE_PF && tid < 120) {
            asm volatile("global_load_dword %0, %1, %2" : "=v"(pfv) : "v"(tid * 128), "s"(pc0 & ~127ull) : "memory");
        }
        v2u e = ald_e(wsd);
        if (B_DELAY) __builtin_amdgcn_s_sleep(B_DELAY);
        STAMP(1);
        v2u fa = {0u, 0u};
        {
            const v4i srd = mk_srd(ckv + (size_t)chunk * 128 * 512, 131072);
#pragma unroll
            for (int i = 0; i < 16; ++i) {
                if (i == FA_AT && wave == 0) fa = ald64(wsd, WS_P1 + (hh * 64 + lane) * 128);
                const int r = (i + 2 * wave + 8 * hh) & 15;
                dma16(srd, (16 * lane) ^ (16 * (2 * (r & 7) + (r >> 3))), (16 * wave + r) * 1024, lds0 + LB_CKV + (16 * wave + r) * 1024);
            }
        }
        STAMP(2);
        asm volatile("s_waitcnt vmcnt(%3)" : "+v"(fa), "+v"(e), "+v"(pfv) : "i"(16 - FA_AT) : "memory");
        const v2u tag = mk_tag(e);
        if (wave == 0 && !flags_ok2(fa, tag)) wait_flags<0>(ws, WS_P1 + (hh * 64 + lane) * 128, (v2u){tag.x | 1u, tag.y});
        barrier_lds();
        if (Q_DELAY) __builtin_amdgcn_s_sleep(Q_DELAY);
        STAMP(3);
        const int fsw = 2 * (n & 7) + (n >> 3), pg = ((g & 1) << 1) | (g >> 1);
        v4u kf[16];
        {
            v4u r[2]; unsigned off[2];
#pragma unroll
            for (int i = 0; i < 2; ++i) { off[i] = WS_QA + b * 32768 + hh * 16384 + i * 8192 + tid * 16; r[i] = ald128(wsd, off[i]); }
            {
                const unsigned o3 = WS_H3 + chunk * 32768 + hh * 16384 + tid * 16;
                st128(ws, CLEAN, o3); st128(ws, CLEAN, o3 + 8192);
                st128(ws, CLEAN, tid < 288 ? WS_H5 + bid * 4608 + tid * 16 : tid < 296 ? WS_ML + chunk * 256 + hh * 128 + (tid - 288) * 16 : 0x7FFFFF00);
            }
            wait_vm<5>();
            STAMP(12);
#pragma unroll
            for (int s = 0; s < 16; ++s) kf[s] = lds128(L, LB_CKV + (16 * wave + n) * 1024 + 16 * ((4 * (s & 3) + pg) ^ fsw) + 256 * (s >> 2));
            WAIT_VM(3, "+v"(r[0]), "+v"(r[1]));
            STAMP(13);
            if (!landed<2>(r)) gather<2, Q_SLP>(ws, off, r);
#pragma unroll
            for (int i = 0; i < 2; ++i) *(LAS v4u*)(L + LB_Q + (wave + 8 * i) * 1024 + 16 * (lane ^ (2 * wave + i))) = r[i];
        }
        STAMP(4);
        barrier_lds();
        STAMP(5);
        f32x4 sc = {0.f, 0.f, 0.f, 0.f};
#pragma unroll
        for (int s = 0; s < 16; ++s) sc = mfma32(kf[s], lds128(L, LB_Q + n * 1024 + 16 * ((4 * (s & 3) + pg) ^ fsw) + 256 * (s >> 2)), sc);
        v4u vf[4][4];
        {
            const int rr = 4 * g + q, fr = 2 * (rr & 7) + (rr >> 3);
#pragma unroll
            for (int t = 0; t < 4; ++t) {
                const unsigned a0 = LB_CKV + rr * 1024 + 256 * (wave >> 1) + 16 * ((8 * (wave & 1) + 2 * t + (p >> 1)) ^ fr) + 8 * (p & 1);
#pragma unroll
                for (int s = 0; s < 4; ++s) vf[t][s] = lds_tr2(L, a0 + s * 32768, a0 + s * 32768 + 16384);
            }
        }
        float mw = x32max(x16max(fmaxf(fmaxf(sc[0], sc[1]), fmaxf(sc[2], sc[3]))));
        if (g == 0) *(LAS float*)(L + LB_MX + n * 32 + wave * 4) = mw;
        barrier_lds();
        STAMP(6);
        float mx;
        {
            const f32x4 m0 = *(LAS f32x4*)(L + LB_MX + n * 32), m1 = *(LAS f32x4*)(L + LB_MX + n * 32 + 16);
            mx = fmaxf(fmaxf(fmaxf(m0[0], m0[1]), fmaxf(m0[2], m0[3])), fmaxf(fmaxf(m1[0], m1[1]), fmaxf(m1[2], m1[3])));
            const float p0 = __builtin_amdgcn_exp2f(sc[0] - mx), p1 = __builtin_amdgcn_exp2f(sc[1] - mx), p2 = __builtin_amdgcn_exp2f(sc[2] - mx), p3 = __builtin_amdgcn_exp2f(sc[3] - mx);
            *(LAS v2u*)(L + LB_P + (wave >> 1) * 1024 + lane * 16 + (wave & 1) * 8) = (v2u){pk(p0, p1), pk(p2, p3)};
            const float lw = x32sum(x16sum((p0 + p1) + (p2 + p3)));
            if (g == 0) *(LAS float*)(L + LB_L + n * 32 + wave * 4) = lw;
        }
        wait_vm<0>();
        barrier_lds();
        if (tid == 0) st64(ws, tag, WS_R3 + bi * 128);
        STAMP(7);
        {
            v4u pf[4];
#pragma unroll
            for (int s = 0; s < 4; ++s) pf[s] = lds128(L, LB_P + s * 1024 + lane * 16);
            if (wave == 7) {
                const f32x4 l0 = *(LAS f32x4*)(L + LB_L + n * 32), l1 = *(LAS f32x4*)(L + LB_L + n * 32 + 16);
                const float ls = ((l0[0] + l0[1]) + (l0[2] + l0[3])) + ((l1[0] + l1[1]) + (l1[2] + l1[3]));
                if (g == 0) st64(ws, (v2u){nos(__float_as_uint(mx)), nos(__float_as_uint(ls))}, WS_ML + chunk * 256 + (hh * 16 + n) * 8);
            }
            const unsigned o3 = WS_H3 + chunk * 32768 + (hh * 16 + n) * 1024 + wave * 128 + pg * 16;
#pragma unroll
            for (int u = 0; u < 2; ++u) {
                f32x4 c0 = {0.f, 0.f, 0.f, 0.f}, c1 = {0.f, 0.f, 0.f, 0.f};
#pragma unroll
                for (int s = 0; s < 4; ++s) { c0 = mfma32(vf[2 * u][s], pf[s], c0); c1 = mfma32(vf[2 * u + 1][s], pf[s], c1); }
                auto s0 = __builtin_amdgcn_permlane16_swap(pk(c0[0], c0[1]), pk(c1[0], c1[1]), false, false);
                auto s1 = __builtin_amdgcn_permlane16_swap(pk(c0[2], c0[3]), pk(c1[2], c1[3]), false, false);
                st128(ws, (v4u){nos(s0[0]), nos(s1[0]), nos(s0[1]), nos(s1[1])}, o3 + u * 64);
            }
        }
        STAMP(8);
        if (wave < 5) {
            const v4i srd = mk_srd(w2 + (size_t)bi * 36 * 576, 36 * 1152);
            const unsigned voff = (lane >> 3) * 1152 + 16 * ((lane & 7) ^ ((((lane >> 4) & 1) | ((wave & 1) << 1)) << 1));
#pragma unroll
            for (int i = 0; i < 9; ++i) dma16(srd, voff, wave * 9216 + i * 128, lds0 + LB_W2 + wave * 9216 + i * 1024);
        } else if (wave >= 6) {
            const int bb = wave - 6, f = lane < 36 ? lane : 35;
            const unsigned o1 = WS_M1 + (bi & ~3) * 1152 + bb * 576 + ((bi & 3) * 36 + f) * 4;
            const float bias = bflo(((const GAS u16*)b1)[bi * 36 + f]);
            unsigned r0, r1, r2, r3;
#pragma unroll 1
            for (unsigned spins = 0;; ++spins) {
                asm volatile("" ::: "memory");
                r0 = ld32(ws, o1); r1 = ld32(ws, o1 + 1152); r2 = ld32(ws, o1 + 2304); r3 = ld32(ws, o1 + 3456);
                if (!__any(umax2(umax2(r0, r1), umax2(r2, r3)) == SENT) || spins >= SPIN_CAP) break;
                __builtin_amdgcn_s_sleep(4);
            }
            const float v = bfr(bfr((__uint_as_float(r0) + __uint_as_float(r1)) + (__uint_as_float(r2) + __uint_as_float(r3))) + bias);
            const float u = 0.7978845608028654f * (v + 0.044715f * v * v * v);
            const float hv = v / (1.f + __expf(-2.f * u));
            if (lane < 48) *(LAS u16*)(L + LB_H + bb * 128 + lane * 2) = lane < 36 ? (u16)pk(hv, hv) : (u16)0;
        }
        STAMP(9);
        wait_vm<0>();
        barrier_lds();
        STAMP(10);
        {
            v4u wf[5][1], hf[1]; v2u wt[5];
            proj576_load<1, true>(L, LB_W2, wave, lane, wf, wt);
            hf[0] = lds128(L, LB_H + (n & 1) * 128 + 16 * g);
            const v2u ht = *(LAS v2u*)(L + LB_H + (n & 1) * 128 + 64 + 8 * g);
            proj576_run<1, true>(ws, bid, wave, lane, wf, wt, hf, ht);
        }
        STAMP(11);
    }
    if (lane == 0) {
        GAS u64* d = (GAS u64*)(wsp + WS_DBG) + (bid * 8 + wave) * 16;
#pragma unroll
        for (int i = 0; i < 16; ++i) d[i] = stamp[i];
    }
}

__global__ void mark_kernel(char* wsp) { u64 t; asm volatile("s_memrealtime %0\n\ts_waitcnt lgkmcnt(0)" : "=s"(t) :: "memory"); *(GAS u64*)(wsp + WS_DBG - 512) = t; }
template <int MODE>
__global__ void spin_kernel(char* wsp) {
    u64 tin; asm volatile("s_memrealtime %0\n\ts_waitcnt lgkmcnt(0)" : "=s"(tin) :: "memory");
    __shared__ u64 sm[3][256];
    const GAS u64* d = (const GAS u64*)(wsp + WS_DBG);
    u64 t0min = ~0ull, v = 0;
    const int bid = threadIdx.x;
    for (int w = 0; w < 8; ++w) {
        const GAS u64* s = d + (bid * 8 + w) * 16;
        t0min = s[0] < t0min ? s[0] : t0min;
        for (int i = 0; i < 16; ++i) v = s[i] > v ? s[i] : v;
    }
    sm[0][bid] = t0min; sm[2][bid] = v;
    __syncthreads();
    if (bid == 0) {
        for (int i = 0; i < 256; ++i) { t0min = sm[0][i] < t0min ? sm[0][i] : t0min; u64 x = sm[2][i]; v = x > v ? x : v; }
        u64 span = MODE == 0 ? tin - v : MODE == 1 ? t0min - *(GAS u64*)(wsp + WS_DBG - 512) : MODE == 2 ? v - t0min : 1;
        if (span > 10000) span = 10000;
        u64 t, t1; asm volatile("s_memrealtime %0\n\ts_waitcnt lgkmcnt(0)" : "=s"(t) :: "memory");
        do { asm volatile("s_memrealtime %0\n\ts_waitcnt lgkmcnt(0)" : "=s"(t1) :: "memory"); } while (t1 < t + span * 100);
    }
}
extern "C" void kernel_launch(void* const* d_in, const int* in_sizes, int n_in, void* d_out, int out_size, void* d_ws, size_t ws_size, hipStream_t stream) {
    fused_decode_kernel<<<256, NT, 0, stream>>>((const u16*)d_in[0], (const u16*)d_in[1], (const u16*)d_in[2], (const u16*)d_in[3], (const u16*)d_in[4],
                                                (const u16*)d_in[5], (const u16*)d_in[7], (const u16*)d_in[8], (const u16*)d_in[9], (const u16*)d_in[10],
                                                (const u16*)d_in[11], (u16*)d_out, (char*)d_ws);
    spin_kernel<3><<<1, 256, 0, stream>>>((char*)d_ws);
}
```

```cpp
#include <hip/hip_runtime.h>
#include <hip/hip_bf16.h>
#include <stdint.h>

typedef unsigned short u16;
typedef unsigned long long u64;
typedef unsigned v4u __attribute__((ext_vector_type(4)));
typedef unsigned v2u __attribute__((ext_vector_type(2)));
typedef int v4i __attribute__((ext_vector_type(4)));
typedef float f32x4 __attribute__((ext_vector_type(4)));
typedef float f32x2 __attribute__((ext_vector_type(2)));
typedef short s16x4 __attribute__((ext_vector_type(4)));
typedef __bf16 bf16x8 __attribute__((ext_vector_type(8)));
typedef __bf16 bf16x2 __attribute__((ext_vector_type(2)));
#define GAS __attribute__((address_space(1)))
#define LAS __attribute__((address_space(3)))
#define DI __device__ __forceinline__
typedef __amdgpu_buffer_rsrc_t rsrc_t;

constexpr int NT = 512;
constexpr unsigned SENT = 0xFFFFFFFFu;
constexpr int SC1 = 16;
constexpr unsigned SPIN_CAP = 1u << 17;
constexpr u64 TAG_STEP = 0x9E3779B97F4A7C16ull;

constexpr int WS_E = 0;
constexpr int WS_P1 = 4096;
constexpr int WS_F2 = WS_P1 + 16384;
constexpr int WS_R3 = WS_F2 + 16384;
constexpr int WS_H1 = WS_R3 + 16384;
constexpr int WS_QA = WS_H1 + 128 * 512;
constexpr int WS_M1 = WS_QA + 65536;
constexpr int WS_ML = WS_M1 + 128 * 1152;
constexpr int WS_H3 = WS_ML + 16384;
constexpr int WS_H5 = WS_H3 + 64 * 32768;
constexpr int WS_END = WS_H5 + 256 * 4608;

constexpr int LA_WQ = 0, LA_WUK = 36864, LA_WO = 0, LA_W1 = 73728, LA_WUV = 115200, LA_CMB = 147968, LA_CTX = 156160, LA_COL = 156416;
constexpr int LB_CKV = 0, LB_W2 = 0, LB_Q = 131072, LB_P = 147456, LB_MX = 151552, LB_L = 152064, LB_H = 152576;
constexpr int LDS_BYTES = 156672;

#define STAMP(i) do {} while (0)

DI v4i mk_srd(const void* p, unsigned bytes) {
    const u64 a = (u64)p;
    return (v4i){(int)(unsigned)a, (int)((unsigned)(a >> 32) & 0xFFFFu), (int)bytes, 0x00020000};
}
DI void dma16(v4i srd, unsigned voff, unsigned soff, unsigned lds) {
    asm volatile("s_mov_b32 m0, %0\n\ts_nop 0\n\tbuffer_load_dwordx4 %1, %2, %3 offen lds" :: "s"(lds), "v"(voff), "s"(srd), "s"(soff) : "memory");
}
DI v4u ald128(v4i srd, unsigned voff) { v4u r; asm volatile("buffer_load_dwordx4 %0, %1, %2, 0 offen sc1" : "=v"(r) : "v"(voff), "s"(srd) : "memory"); return r; }
DI v4u ald128p(v4i srd, unsigned voff) { v4u r; asm volatile("buffer_load_dwordx4 %0, %1, %2, 0 offen" : "=v"(r) : "v"(voff), "s"(srd) : "memory"); return r; }
DI v2u ald64p(v4i srd, unsigned voff) { v2u r; asm volatile("buffer_load_dwordx2 %0, %1, %2, 0 offen" : "=v"(r) : "v"(voff), "s"(srd) : "memory"); return r; }
DI v2u ald_e(v4i srd) { v2u r; asm volatile("buffer_load_dwordx2 %0, off, %1, 0 sc1" : "=v"(r) : "s"(srd) : "memory"); return r; }
#define WAIT_VM(N, ...) asm volatile("s_waitcnt vmcnt(" #N ")" : __VA_ARGS__ :: "memory")
DI v2u ald64(v4i srd, unsigned voff) { v2u r; asm volatile("buffer_load_dwordx2 %0, %1, %2, 0 offen sc1" : "=v"(r) : "v"(voff), "s"(srd) : "memory"); return r; }
DI bool flags_ok(v2u f, v2u tag) { return __all((f.x == tag.x) & (f.y == tag.y)); }
DI bool flags_ok2(v2u f, v2u tag) { return __all(((f.x | 1u) == (tag.x | 1u)) & (f.y == tag.y)); }
DI v2u mk_tag(v2u e) { const u64 t = (((u64)e.y << 32) | e.x) + TAG_STEP; return (v2u){(unsigned)t & ~1u, (unsigned)(t >> 32)}; }
template <int N> DI void wait_vm() { asm volatile("s_waitcnt vmcnt(%0)" :: "i"(N) : "memory"); }
DI void barrier_lds() { asm volatile("s_waitcnt lgkmcnt(0)\n\ts_barrier" ::: "memory"); }
DI f32x4 mfma32(v4u a, v4u b, f32x4 c) { return __builtin_amdgcn_mfma_f32_16x16x32_bf16(__builtin_bit_cast(bf16x8, a), __builtin_bit_cast(bf16x8, b), c, 0, 0, 0); }
DI f32x4 mfma16(v2u a, v2u b, f32x4 c) { return mfma32((v4u){a.x, a.y, 0u, 0u}, (v4u){b.x, b.y, 0u, 0u}, c); }
DI v2u lds_tr(LAS char* L, unsigned off) { return __builtin_bit_cast(v2u, __builtin_amdgcn_ds_read_tr16_b64_v4i16((LAS s16x4*)(L + off))); }
DI v4u lds_tr2(LAS char* L, unsigned o0, unsigned o1) { const v2u a = lds_tr(L, o0), b = lds_tr(L, o1); return (v4u){a.x, a.y, b.x, b.y}; }
DI v4u lds128(LAS char* L, unsigned off) { return *(LAS v4u*)(L + off); }
DI unsigned pk(float lo, float hi) { return __builtin_bit_cast(unsigned, __builtin_convertvector((f32x2){lo, hi}, bf16x2)); }
DI unsigned nos(unsigned v) { return v < 0xFFFFFFFEu ? v : 0xFFFFFFFEu; }
DI float bflo(unsigned v) { return __uint_as_float(v << 16); }
DI float bfhi(unsigned v) { return __uint_as_float(v & 0xFFFF0000u); }
DI float bfr(float f) { return bflo(pk(f, f)); }
template <int CTRL> DI float dpp(float x) { return __builtin_bit_cast(float, __builtin_amdgcn_mov_dpp(__builtin_bit_cast(int, x), CTRL, 0xf, 0xf, true)); }
constexpr int XOR1 = 0xB1, XOR2 = 0x4E, ROR4 = 0x124, ROR8 = 0x128;
DI float x16max(float x) { auto s = __builtin_amdgcn_permlane16_swap(__float_as_uint(x), __float_as_uint(x), false, false); return fmaxf(__uint_as_float(s[0]), __uint_as_float(s[1])); }
DI float x32max(float x) { auto s = __builtin_amdgcn_permlane32_swap(__float_as_uint(x), __float_as_uint(x), false, false); return fmaxf(__uint_as_float(s[0]), __uint_as_float(s[1])); }
DI float x16sum(float x) { auto s = __builtin_amdgcn_permlane16_swap(__float_as_uint(x), __float_as_uint(x), false, false); return __uint_as_float(s[0]) + __uint_as_float(s[1]); }
DI float x32sum(float x) { auto s = __builtin_amdgcn_permlane32_swap(__float_as_uint(x), __float_as_uint(x), false, false); return __uint_as_float(s[0]) + __uint_as_float(s[1]); }
DI unsigned umax2(unsigned a, unsigned b) { return a > b ? a : b; }
DI unsigned umax4(v4u v) { return umax2(umax2(v.x, v.y), umax2(v.z, v.w)); }
DI void st128(rsrc_t rs, v4u v, unsigned off) { __builtin_amdgcn_raw_buffer_store_b128(v, rs, off, 0, SC1); }
DI void st64(rsrc_t rs, v2u v, unsigned off) { __builtin_amdgcn_raw_buffer_store_b64(v, rs, off, 0, SC1); }
DI v4u ld128(rsrc_t rs, unsigned off) { return __builtin_amdgcn_raw_buffer_load_b128(rs, off, 0, SC1); }
DI v2u ld64(rsrc_t rs, unsigned off) { return __builtin_amdgcn_raw_buffer_load_b64(rs, off, 0, SC1); }
DI unsigned ld32(rsrc_t rs, unsigned off) { return __builtin_amdgcn_raw_buffer_load_b32(rs, off, 0, SC1); }

template <int SLP, bool ANY = false> DI void wait_flags(rsrc_t rs, unsigned off, v2u tag) {
#pragma unroll 1
    for (unsigned spins = 0; spins < SPIN_CAP; ++spins) {
        asm volatile("" ::: "memory");
        const v2u f = ld64(rs, off);
        if (ANY ? flags_ok2(f, tag) : flags_ok(f, tag)) break;
        if (SLP) __builtin_amdgcn_s_sleep(SLP);
    }
    asm volatile("" ::: "memory");
}
template <int N> DI bool landed(const v4u (&r)[N]) {
    unsigned m = 0;
#pragma unroll
    for (int i = 0; i < N; ++i) m = umax2(m, umax4(r[i]));
    return !__any(m == SENT);
}
template <int N, int SLP = 0> DI void gather(rsrc_t rs, const unsigned (&off)[N], v4u (&r)[N]) {
#pragma unroll 1
    for (unsigned spins = 0;; ++spins) {
        asm volatile("" ::: "memory");
#pragma unroll
        for (int i = 0; i < N; ++i) r[i] = ld128(rs, off[i]);
        unsigned m = 0;
#pragma unroll
        for (int i = 0; i < N; ++i) m = umax2(m, umax4(r[i]));
        if (!__any(m == SENT) || spins >= SPIN_CAP) break;
        if (SLP) __builtin_amdgcn_s_sleep(SLP);
    }
}

template <int NS32, bool TAIL16>
DI void proj576_load(LAS char* L, int base, int wave, int lane, v4u (&wf)[5][NS32], v2u (&wt)[5]) {
    const int g = lane >> 4, q = (lane >> 2) & 3, p = lane & 3, tt = wave & 3, g0 = wave >> 2;
    const int x = (q >> 1) | ((g & 1) << 1);
    const unsigned a0 = base + g * 9216 + g0 * 1024 + q * 128 + 16 * ((2 * tt + (p >> 1)) ^ (x << 1)) + 8 * (p & 1);
    const unsigned at = base + 4 * NS32 * 9216 + g0 * 1024 + q * 128 + 16 * ((2 * tt + (p >> 1)) ^ ((q >> 1) << 1)) + 8 * (p & 1);
#pragma unroll
    for (int i = 0; i < 5; ++i) {
        if (i < 4 || g0 == 0) {
#pragma unroll
            for (int s = 0; s < NS32; ++s) wf[i][s] = lds_tr2(L, a0 + s * 36864 + i * 2048, a0 + s * 36864 + i * 2048 + 512);
            if (TAIL16) wt[i] = lds_tr(L, at + i * 2048);
        }
    }
}
template <int NS32, bool TAIL16>
DI void proj576_run(rsrc_t ws, int bid, int wave, int lane, const v4u (&wf)[5][NS32], const v2u (&wt)[5], const v4u (&bf)[NS32], v2u bt) {
    const int g = lane >> 4, n = lane & 15, tt = wave & 3, g0 = wave >> 2;
    const unsigned o0 = WS_H5 + bid * 4608 + (n & 1) * 2304 + (g0 * 64 + tt * 16 + 4 * g) * 4;
#pragma unroll
    for (int i = 0; i < 5; ++i) {
        if (i < 4 || g0 == 0) {
            f32x4 c = {0.f, 0.f, 0.f, 0.f};
#pragma unroll
            for (int s = 0; s < NS32; ++s) c = mfma32(wf[i][s], bf[s], c);
            if (TAIL16) c = mfma16(wt[i], bt, c);
            const v4u v = {nos(__float_as_uint(c[0])), nos(__float_as_uint(c[1])), nos(__float_as_uint(c[2])), nos(__float_as_uint(c[3]))};
            if (n < 2) st128(ws, v, o0 + i * 512);
        }
    }
}

#define WO_BEFORE 4
#define WO_MID 9
#define A_DELAY 0
#define H1_SLP 2
#define Q_SLP 4
#define FA_AT 10
#define B_DELAY 16
#define Q_DELAY 0
#define H1_DELAY 0
#define CODE_PF 1
__global__ void __launch_bounds__(NT, 2)
fused_decode_kernel(const u16* __restrict__ xp, const u16* __restrict__ ckv, const u16* __restrict__ wq, const u16* __restrict__ wuk,
                    const u16* __restrict__ wuv, const u16* __restrict__ wo, const u16* __restrict__ w1, const u16* __restrict__ b1,
                    const u16* __restrict__ w2, const u16* __restrict__ b2, const u16* __restrict__ gw, u16* __restrict__ out, char* wsp) {
    __shared__ __attribute__((aligned(1024))) char smem[LDS_BYTES];
    LAS char* L = (LAS char*)smem;
    const unsigned lds0 = (unsigned)(size_t)L;
    STAMP(0);
    u64 pc0; asm volatile("s_getpc_b64 %0" : "=s"(pc0));
    asm volatile("" :: "s"(xp), "s"(ckv), "s"(wq), "s"(wuk), "s"(wuv), "s"(wo), "s"(w1), "s"(b1), "s"(w2), "s"(b2), "s"(gw), "s"(out), "s"(wsp));
    const int bid = blockIdx.x, tid = threadIdx.x, lane = tid & 63;
    const int wave = __builtin_amdgcn_readfirstlane(tid >> 6);
    const int xcd = bid & 7, slot = bid >> 3;
    const int g = lane >> 4, n = lane & 15, q = (lane >> 2) & 3, p = lane & 3;
    const rsrc_t ws = __builtin_amdgcn_make_buffer_rsrc(wsp, 0, WS_END, 0x00020000);
    const v4i wsd = mk_srd(wsp, WS_END);
    const v4u CLEAN = {SENT, SENT, SENT, SENT};

    if (__builtin_expect(slot < 16, 1)) {
        const int a = xcd * 16 + slot, h = a >> 2, j = a & 3;
        asm volatile("s_nop 4" ::: "memory");
        v2u e = ald_e(wsd);
        if (wave == 0) st128(ws, CLEAN, lane < 32 ? WS_H1 + a * 512 + lane * 16 : WS_QA + (g & 1) * 32768 + h * 1024 + j * 256 + n * 16);
        {
            const v4i srd = mk_srd(wq + (size_t)j * 144 * 4096 + h * 128, 143 * 8192 + 256);
            const unsigned voff = g * 8192 + 16 * (n ^ ((4 * ((wave >> 1) & 1) + g) << 1));
#pragma unroll
            for (int i = 0; i < 5; ++i)
                if (i < 4 || wave < 4) dma16(srd, voff, (wave + 8 * i) * 32768, lds0 + LA_WQ + (wave + 8 * i) * 1024);
        }
        v4u xf[4]; v2u xt;
        {
            const v4i srd = mk_srd(xp, 2304);
            const unsigned xo = (n & 1) * 1152 + j * 288;
#pragma unroll
            for (int s = 0; s < 4; ++s) xf[s] = ald128p(srd, xo + 64 * s + 16 * g);
            xt = ald64p(srd, xo + 256 + 8 * g);
        }
        {
            const v4i srd = mk_srd(wuk + (size_t)j * 128 * 4096 + h * 128, 127 * 8192 + 256);
#pragma unroll
            for (int i = 0; i < 4; ++i)
                dma16(srd, g * 8192 + 16 * (n ^ (4 * i + g)), (16 * wave + 4 * i) * 8192, lds0 + LA_WUK + (16 * wave + 4 * i) * 256);
        }
        WAIT_VM(4, "+v"(e), "+v"(xf[0]), "+v"(xf[1]), "+v"(xf[2]), "+v"(xf[3]), "+v"(xt));
        const v2u tag = mk_tag(e);
        STAMP(1);
        barrier_lds();
        if (tid == 0) st64(ws, tag, WS_P1 + a * 128);
        STAMP(14);
        {
            const unsigned cw = 2 * wave + (p >> 1);
            const unsigned a0 = LA_WQ + (8 * g + q) * 256 + 16 * (cw ^ ((q + 4 * (g & 1)) << 1)) + 8 * (p & 1);
            const unsigned at = LA_WQ + (128 + 4 * g + q) * 256 + 16 * (cw ^ ((q + 4 * (g >> 1)) << 1)) + 8 * (p & 1);
            f32x4 c = {0.f, 0.f, 0.f, 0.f};
#pragma unroll
            for (int s = 0; s < 4; ++s) c = mfma32(lds_tr2(L, a0 + s * 8192, a0 + s * 8192 + 1024), xf[s], c);
            c = mfma16(lds_tr(L, at), xt, c);
            if (n < 2) st64(ws, (v2u){nos(pk(c[0], c[1])), nos(pk(c[2], c[3]))}, WS_H1 + a * 512 + wave * 64 + n * 32 + g * 8);
        }
        st128(ws, CLEAN, tid < 72 ? WS_M1 + a * 1152 + tid * 16 : tid < 360 ? WS_H5 + bid * 4608 + (tid - 72) * 16 : 0x7FFFFF00);
        STAMP(2);
        wait_flags<0, true>(ws, WS_P1 + (4 * h + p) * 128, tag);
        if (H1_DELAY) __builtin_amdgcn_s_sleep(H1_DELAY);
        STAMP(3);
        {
            v4u uf[4], qf[4]; unsigned off[4];
#pragma unroll
            for (int s = 0; s < 4; ++s) off[s] = WS_H1 + (4 * h + ((n >> 1) & 3)) * 512 + (2 * s + (g >> 1)) * 64 + (n & 1) * 32 + (g & 1) * 16;
            gather<4, H1_SLP>(ws, off, qf);
            if (tid == 0) st64(ws, (v2u){tag.x | 1u, tag.y}, WS_P1 + a * 128);
            STAMP(4);
#pragma unroll
            for (int s = 0; s < 4; ++s) uf[s] = lds128(L, LA_WUK + (16 * wave + n) * 256 + 16 * ((4 * s + g) ^ n));
            f32x4 c = {0.f, 0.f, 0.f, 0.f};
#pragma unroll
            for (int s = 0; s < 4; ++s) c = mfma32(uf[s], qf[s], c);
            constexpr float SC = 0.08838834764831845f * 1.4426950408889634f;
#pragma unroll
            for (int r = 0; r < 4; ++r) { c[r] += dpp<XOR2>(c[r]); c[r] += dpp<ROR4>(c[r]); c[r] *= SC; }
            if (n < 2) st64(ws, (v2u){nos(pk(c[0], c[1])), nos(pk(c[2], c[3]))}, WS_QA + n * 32768 + h * 1024 + (128 * j + 16 * wave + 4 * g) * 2);
        }
        STAMP(5);
        const int cs = j >> 1, ds = j & 1;
        if (A_DELAY) __builtin_amdgcn_s_sleep(A_DELAY);
        {
            const v4i srd = mk_srd(w1 + (size_t)j * 144 * 4608 + h * 144, 143 * 9216 + 288);
            const unsigned r3 = lane / 18, voff = r3 * 9216 + (lane - 18 * r3) * 16;
            if (lane < 54) {
#pragma unroll
                for (int i = 0; i < 6; ++i) dma16(srd, voff, (wave + 8 * i) * 27648, lds0 + LA_W1 + (wave + 8 * i) * 864);
            }
        }
        const unsigned sub_c = 16 * ((lane & 7) ^ ((((lane >> 4) & 1) | ((wave & 1) << 1)) << 1));
        {
            const v4i srd = mk_srd(wuv + (size_t)cs * 256 * 4096 + h * 128 + ds * 64, 255 * 8192 + 128);
#pragma unroll
            for (int i = 0; i < 4; ++i) dma16(srd, (lane >> 3) * 8192 + sub_c, (wave + 8 * i) * 65536, lds0 + LA_WUV + (wave + 8 * i) * 1024);
        }
        STAMP(6);
        wait_vm<4>();
        barrier_lds();
        if (tid == 0) st64(ws, tag, WS_F2 + a * 128);
        STAMP(7);
        {
            const unsigned a0 = LA_W1 + (8 * g + q) * 288 + 8 * p + 32 * wave, at = LA_W1 + (128 + 4 * g + q) * 288 + 8 * p + 32 * wave;
#pragma unroll
            for (int t = 0; t < 2; ++t) {
                if (t == 0 || wave == 0) {
                    f32x4 c = {0.f, 0.f, 0.f, 0.f};
#pragma unroll
                    for (int s = 0; s < 4; ++s) c = mfma32(lds_tr2(L, a0 + t * 256 + s * 9216, a0 + t * 256 + s * 9216 + 1152), xf[s], c);
                    c = mfma16(lds_tr(L, at + t * 256), xt, c);
                    const v4u v = {nos(__float_as_uint(c[0])), nos(__float_as_uint(c[1])), nos(__float_as_uint(c[2])), nos(__float_as_uint(c[3]))};
                    if (n < 2) st128(ws, v, WS_M1 + a * 1152 + n * 576 + (16 * wave + 128 * t + 4 * g) * 4);
                }
            }
        }
        STAMP(8);
        const int b = wave >> 2, hf = lane >> 5, li = lane & 31, k0 = 32 * b + 8 * (wave & 3) + 4 * hf;
        v4u r[4]; v2u ml[4]; unsigned off[4];
#pragma unroll
        for (int i = 0; i < 4; ++i) off[i] = WS_H3 + (k0 + i) * 32768 + h * 1024 + cs * 512 + li * 16;
        const unsigned offm = WS_ML + k0 * 256 + h * 8;
        {
            const v4i srd = mk_srd(wo + (size_t)(h * 128 + ds * 64) * 576, 64 * 1152);
#pragma unroll
            for (int i = 0; i < WO_BEFORE; ++i) dma16(srd, (lane >> 3) * 1152 + sub_c, wave * 9216 + i * 128, lds0 + LA_WO + wave * 9216 + i * 1024);
            if (wave == 0) wait_flags<0>(ws, WS_R3 + ((h >> 4) * 64 + lane) * 128, tag);
            barrier_lds();
            STAMP(9);
#pragma unroll
            for (int i = 0; i < 4; ++i) { r[i] = ald128(wsd, off[i]); ml[i] = ald64(wsd, offm + i * 256); }
#pragma unroll
            for (int i = WO_BEFORE; i < WO_MID; ++i) dma16(srd, (lane >> 3) * 1152 + sub_c, wave * 9216 + i * 128, lds0 + LA_WO + wave * 9216 + i * 1024);
            asm volatile("s_waitcnt vmcnt(%8)" : "+v"(r[0]), "+v"(r[1]), "+v"(r[2]), "+v"(r[3]), "+v"(ml[0]), "+v"(ml[1]), "+v"(ml[2]), "+v"(ml[3]) : "i"(WO_MID - WO_BEFORE) : "memory");
            STAMP(13);
#pragma unroll 1
            for (unsigned spins = 0; spins < SPIN_CAP; ++spins) {
                unsigned m = umax2(umax2(umax4(r[0]), umax4(r[1])), umax2(umax4(r[2]), umax4(r[3])));
                m = umax2(m, umax2(umax4((v4u){ml[0].x, ml[0].y, ml[1].x, ml[1].y}), umax4((v4u){ml[2].x, ml[2].y, ml[3].x, ml[3].y})));
                if (!__any(m == SENT)) break;
                asm volatile("" ::: "memory");
#pragma unroll
                for (int i = 0; i < 4; ++i) { r[i] = ld128(ws, off[i]); ml[i] = ld64(ws, offm + i * 256); }
            }
            STAMP(10);
            const float mx = fmaxf(fmaxf(__uint_as_float(ml[0].x), __uint_as_float(ml[1].x)), fmaxf(__uint_as_float(ml[2].x), __uint_as_float(ml[3].x)));
            float wk[4];
#pragma unroll
            for (int i = 0; i < 4; ++i) wk[i] = __builtin_amdgcn_exp2f(__uint_as_float(ml[i].x) - mx);
            float acc[8];
#pragma unroll
            for (int k = 0; k < 4; ++k) { acc[2 * k] = wk[0] * bflo(r[0][k]); acc[2 * k + 1] = wk[0] * bfhi(r[0][k]); }
#pragma unroll
            for (int i = 1; i < 4; ++i)
#pragma unroll
                for (int k = 0; k < 4; ++k) { acc[2 * k] = fmaf(wk[i], bflo(r[i][k]), acc[2 * k]); acc[2 * k + 1] = fmaf(wk[i], bfhi(r[i][k]), acc[2 * k + 1]); }
            const int col = 4 * (wave & 3) + 2 * hf + b;
            *(LAS v4u*)(L + LA_CMB + col * 512 + 16 * (li ^ col)) = (v4u){pk(acc[0], acc[1]), pk(acc[2], acc[3]), pk(acc[4], acc[5]), pk(acc[6], acc[7])};
            const float ls = (wk[0] * __uint_as_float(ml[0].y) + wk[1] * __uint_as_float(ml[1].y)) + (wk[2] * __uint_as_float(ml[2].y) + wk[3] * __uint_as_float(ml[3].y));
            if (li == 0) *(LAS f32x2*)(L + LA_COL + 8 * col) = (f32x2){mx, ls};
#pragma unroll
            for (int i = WO_MID; i < 9; ++i) dma16(srd, (lane >> 3) * 1152 + sub_c, wave * 9216 + i * 128, lds0 + LA_WO + wave * 9216 + i * 1024);
        }
        barrier_lds();
        if (wave < 4) {
            const unsigned a0 = LA_WUV + g * 1024 + q * 128 + 16 * ((2 * wave + (p >> 1)) ^ (((q >> 1) | ((g & 1) << 1)) << 1)) + 8 * (p & 1);
            f32x4 c = {0.f, 0.f, 0.f, 0.f};
#pragma unroll
            for (int s = 0; s < 8; ++s) c = mfma32(lds_tr2(L, a0 + s * 4096, a0 + s * 4096 + 512), lds128(L, LA_CMB + n * 512 + 16 * ((4 * s + g) ^ n)), c);
            const f32x2 cm = *(LAS f32x2*)(L + LA_COL + 8 * n);
            float mx = fmaxf(cm[0], dpp<XOR2>(cm[0])); mx = fmaxf(mx, dpp<ROR4>(mx)); mx = fmaxf(mx, dpp<ROR8>(mx));
            const float wc = __builtin_amdgcn_exp2f(cm[0] - mx);
            float ls = wc * cm[1]; ls += dpp<XOR2>(ls); ls += dpp<ROR4>(ls); ls += dpp<ROR8>(ls);
            const float sc = wc / ls;
#pragma unroll
            for (int r = 0; r < 4; ++r) { c[r] *= sc; c[r] += dpp<XOR2>(c[r]); c[r] += dpp<ROR4>(c[r]); c[r] += dpp<ROR8>(c[r]); }
            if (n < 2) *(LAS v2u*)(L + LA_CTX + n * 128 + (16 * wave + 4 * g) * 2) = (v2u){pk(c[0], c[1]), pk(c[2], c[3])};
        }
        wait_vm<0>();
        barrier_lds();
        STAMP(11);
        {
            v4u cf[2], of[5][2]; v2u ot[5];
#pragma unroll
            for (int s = 0; s < 2; ++s) cf[s] = lds128(L, LA_CTX + (n & 1) * 128 + 64 * s + 16 * g);
            proj576_load<2, false>(L, LA_WO, wave, lane, of, ot);
            proj576_run<2, false>(ws, bid, wave, lane, of, ot, cf, (v2u){0u, 0u});
        }
        STAMP(12);
        if (bid < 36) {
            float gate = 0.f, bias = 0.f;
            const int o = bid * 32 + (lane & 31), m = o - (bid / 18) * 576;
            if (wave == 0) {
                const GAS u16* xr = (const GAS u16*)xp + (bid / 18) * 576;
                float s = 0.f;
#pragma unroll
                for (int i = 0; i < 9; ++i) s += bfr(bflo(xr[lane + 64 * i]) * bflo(((const GAS u16*)gw)[lane + 64 * i]));
                s += dpp<XOR1>(s); s += dpp<XOR2>(s); s += dpp<ROR4>(s); s += dpp<ROR8>(s); s = x32sum(x16sum(s));
                gate = bfr(1.f / (1.f + __expf(-bfr(s))));
                bias = bflo(((const GAS u16*)b2)[m]);
            }
            if (wave < 4) wait_flags<2>(ws, WS_F2 + (wave * 64 + lane) * 128, tag);
            wait_vm<0>();
            barrier_lds();
            v4u r5[4]; unsigned o5[4];
#pragma unroll
            for (int i = 0; i < 4; ++i) o5[i] = WS_H5 + ((lane >> 3) + 8 * wave + 64 * i) * 4608 + bid * 128 + (lane & 7) * 16;
            gather<4>(ws, o5, r5);
            float t[4];
#pragma unroll
            for (int k = 0; k < 4; ++k) {
                const float sa = __uint_as_float(r5[0][k]) + __uint_as_float(r5[1][k]), sb = __uint_as_float(r5[2][k]) + __uint_as_float(r5[3][k]);
                auto s = __builtin_amdgcn_permlane32_swap(__float_as_uint(sa), __float_as_uint(sb), false, false);
                t[k] = __uint_as_float(s[0]) + __uint_as_float(s[1]);
                t[k] = x16sum(t[k]);
                t[k] += dpp<ROR8>(t[k]);
            }
            if ((lane & 24) == 0) *(LAS f32x4*)(L + LA_CMB + wave * 256 + (lane >> 5) * 128 + (lane & 7) * 16) = (f32x4){t[0], t[1], t[2], t[3]};
            barrier_lds();
            if (wave == 0) {
                float s = 0.f;
#pragma unroll
                for (int w = 0; w < 8; ++w) s += *(LAS float*)(L + LA_CMB + w * 256 + lane * 4);
                auto sw = __builtin_amdgcn_permlane32_swap(__float_as_uint(s), __float_as_uint(s), false, false);
                const float av = bfr(__uint_as_float(sw[0]));
                const float bv = bfr(bfr(__uint_as_float(sw[1])) + bias);
                const float res = bfr(gate * av) + bfr(bfr(1.f - gate) * bv);
                if (lane < 32) ((GAS u16*)out)[o] = (u16)pk(res, res);
                if (bid == 0 && lane == 0) st64(ws, tag, WS_E);
            }
            STAMP(15);
        }
    } else {
        const int sb = slot - 16, chunk = xcd * 8 + (sb >> 1), hh = sb & 1, b = chunk >> 5, bi = hh * 64 + chunk;
        asm volatile("s_nop 4" ::: "memory");
        unsigned pfv = 0;
        if (CODE_PF && tid < 120) {
            asm volatile("global_load_dword %0, %1, %2" : "=v"(pfv) : "v"(tid * 128), "s"(pc0 & ~127ull) : "memory");
        }
        v2u e = ald_e(wsd);
        if (B_DELAY) __builtin_amdgcn_s_sleep(B_DELAY);
        STAMP(1);
        v2u fa = {0u, 0u};
        {
            const v4i srd = mk_srd(ckv + (size_t)chunk * 128 * 512, 131072);
#pragma unroll
            for (int i = 0; i < 16; ++i) {
                if (i == FA_AT && wave == 0) fa = ald64(wsd, WS_P1 + (hh * 64 + lane) * 128);
                const int r = (i + 2 * wave + 8 * hh) & 15;
                dma16(srd, (16 * lane) ^ (16 * (2 * (r & 7) + (r >> 3))), (16 * wave + r) * 1024, lds0 + LB_CKV + (16 * wave + r) * 1024);
            }
        }
        STAMP(2);
        asm volatile("s_waitcnt vmcnt(%3)" : "+v"(fa), "+v"(e), "+v"(pfv) : "i"(16 - FA_AT) : "memory");
        const v2u tag = mk_tag(e);
        if (wave == 0 && !flags_ok2(fa, tag)) wait_flags<0>(ws, WS_P1 + (hh * 64 + lane) * 128, (v2u){tag.x | 1u, tag.y});
        barrier_lds();
        if (Q_DELAY) __builtin_amdgcn_s_sleep(Q_DELAY);
        STAMP(3);
        const int fsw = 2 * (n & 7) + (n >> 3), pg = ((g & 1) << 1) | (g >> 1);
        v4u kf[16];
        {
            v4u r[2]; unsigned off[2];
#pragma unroll
            for (int i = 0; i < 2; ++i) { off[i] = WS_QA + b * 32768 + hh * 16384 + i * 8192 + tid * 16; r[i] = ald128(wsd, off[i]); }
            {
                const unsigned o3 = WS_H3 + chunk * 32768 + hh * 16384 + tid * 16;
                st128(ws, CLEAN, o3); st128(ws, CLEAN, o3 + 8192);
                st128(ws, CLEAN, tid < 288 ? WS_H5 + bid * 4608 + tid * 16 : tid < 296 ? WS_ML + chunk * 256 + hh * 128 + (tid - 288) * 16 : 0x7FFFFF00);
            }
            wait_vm<5>();
            STAMP(12);
#pragma unroll
            for (int s = 0; s < 16; ++s) kf[s] = lds128(L, LB_CKV + (16 * wave + n) * 1024 + 16 * ((4 * (s & 3) + pg) ^ fsw) + 256 * (s >> 2));
            WAIT_VM(3, "+v"(r[0]), "+v"(r[1]));
            STAMP(13);
            if (!landed<2>(r)) gather<2, Q_SLP>(ws, off, r);
#pragma unroll
            for (int i = 0; i < 2; ++i) *(LAS v4u*)(L + LB_Q + (wave + 8 * i) * 1024 + 16 * (lane ^ (2 * wave + i))) = r[i];
        }
        STAMP(4);
        barrier_lds();
        STAMP(5);
        f32x4 sc = {0.f, 0.f, 0.f, 0.f};
#pragma unroll
        for (int s = 0; s < 16; ++s) sc = mfma32(kf[s], lds128(L, LB_Q + n * 1024 + 16 * ((4 * (s & 3) + pg) ^ fsw) + 256 * (s >> 2)), sc);
        v4u vf[4][4];
        {
            const int rr = 4 * g + q, fr = 2 * (rr & 7) + (rr >> 3);
#pragma unroll
            for (int t = 0; t < 4; ++t) {
                const unsigned a0 = LB_CKV + rr * 1024 + 256 * (wave >> 1) + 16 * ((8 * (wave & 1) + 2 * t + (p >> 1)) ^ fr) + 8 * (p & 1);
#pragma unroll
                for (int s = 0; s < 4; ++s) vf[t][s] = lds_tr2(L, a0 + s * 32768, a0 + s * 32768 + 16384);
            }
        }
        float mw = x32max(x16max(fmaxf(fmaxf(sc[0], sc[1]), fmaxf(sc[2], sc[3]))));
        if (g == 0) *(LAS float*)(L + LB_MX + n * 32 + wave * 4) = mw;
        barrier_lds();
        STAMP(6);
        float mx;
        {
            const f32x4 m0 = *(LAS f32x4*)(L + LB_MX + n * 32), m1 = *(LAS f32x4*)(L + LB_MX + n * 32 + 16);
            mx = fmaxf(fmaxf(fmaxf(m0[0], m0[1]), fmaxf(m0[2], m0[3])), fmaxf(fmaxf(m1[0], m1[1]), fmaxf(m1[2], m1[3])));
            const float p0 = __builtin_amdgcn_exp2f(sc[0] - mx), p1 = __builtin_amdgcn_exp2f(sc[1] - mx), p2 = __builtin_amdgcn_exp2f(sc[2] - mx), p3 = __builtin_amdgcn_exp2f(sc[3] - mx);
            *(LAS v2u*)(L + LB_P + (wave >> 1) * 1024 + lane * 16 + (wave & 1) * 8) = (v2u){pk(p0, p1), pk(p2, p3)};
            const float lw = x32sum(x16sum((p0 + p1) + (p2 + p3)));
            if (g == 0) *(LAS float*)(L + LB_L + n * 32 + wave * 4) = lw;
        }
        wait_vm<0>();
        barrier_lds();
        if (tid == 0) st64(ws, tag, WS_R3 + bi * 128);
        STAMP(7);
        {
            v4u pf[4];
#pragma unroll
            for (int s = 0; s < 4; ++s) pf[s] = lds128(L, LB_P + s * 1024 + lane * 16);
            if (wave == 7) {
                const f32x4 l0 = *(LAS f32x4*)(L + LB_L + n * 32), l1 = *(LAS f32x4*)(L + LB_L + n * 32 + 16);
                const float ls = ((l0[0] + l0[1]) + (l0[2] + l0[3])) + ((l1[0] + l1[1]) + (l1[2] + l1[3]));
                if (g == 0) st64(ws, (v2u){nos(__float_as_uint(mx)), nos(__float_as_uint(ls))}, WS_ML + chunk * 256 + (hh * 16 + n) * 8);
            }
            const unsigned o3 = WS_H3 + chunk * 32768 + (hh * 16 + n) * 1024 + wave * 128 + pg * 16;
#pragma unroll
            for (int u = 0; u < 2; ++u) {
                f32x4 c0 = {0.f, 0.f, 0.f, 0.f}, c1 = {0.f, 0.f, 0.f, 0.f};
#pragma unroll
                for (int s = 0; s < 4; ++s) { c0 = mfma32(vf[2 * u][s], pf[s], c0); c1 = mfma32(vf[2 * u + 1][s], pf[s], c1); }
                auto s0 = __builtin_amdgcn_permlane16_swap(pk(c0[0], c0[1]), pk(c1[0], c1[1]), false, false);
                auto s1 = __builtin_amdgcn_permlane16_swap(pk(c0[2], c0[3]), pk(c1[2], c1[3]), false, false);
                st128(ws, (v4u){nos(s0[0]), nos(s1[0]), nos(s0[1]), nos(s1[1])}, o3 + u * 64);
            }
        }
        STAMP(8);
        if (wave < 5) {
            const v4i srd = mk_srd(w2 + (size_t)bi * 36 * 576, 36 * 1152);
            const unsigned voff = (lane >> 3) * 1152 + 16 * ((lane & 7) ^ ((((lane >> 4) & 1) | ((wave & 1) << 1)) << 1));
#pragma unroll
            for (int i = 0; i < 9; ++i) dma16(srd, voff, wave * 9216 + i * 128, lds0 + LB_W2 + wave * 9216 + i * 1024);
        } else if (wave >= 6) {
            const int bb = wave - 6, f = lane < 36 ? lane : 35;
            const unsigned o1 = WS_M1 + (bi & ~3) * 1152 + bb * 576 + ((bi & 3) * 36 + f) * 4;
            const float bias = bflo(((const GAS u16*)b1)[bi * 36 + f]);
            unsigned r0, r1, r2, r3;
#pragma unroll 1
            for (unsigned spins = 0;; ++spins) {
                asm volatile("" ::: "memory");
                r0 = ld32(ws, o1); r1 = ld32(ws, o1 + 1152); r2 = ld32(ws, o1 + 2304); r3 = ld32(ws, o1 + 3456);
                if (!__any(umax2(umax2(r0, r1), umax2(r2, r3)) == SENT) || spins >= SPIN_CAP) break;
                __builtin_amdgcn_s_sleep(4);
            }
            const float v = bfr(bfr((__uint_as_float(r0) + __uint_as_float(r1)) + (__uint_as_float(r2) + __uint_as_float(r3))) + bias);
            const float u = 0.7978845608028654f * (v + 0.044715f * v * v * v);
            const float hv = v / (1.f + __expf(-2.f * u));
            if (lane < 48) *(LAS u16*)(L + LB_H + bb * 128 + lane * 2) = lane < 36 ? (u16)pk(hv, hv) : (u16)0;
        }
        STAMP(9);
        wait_vm<0>();
        barrier_lds();
        STAMP(10);
        {
            v4u wf[5][1], hf[1]; v2u wt[5];
            proj576_load<1, true>(L, LB_W2, wave, lane, wf, wt);
            hf[0] = lds128(L, LB_H + (n & 1) * 128 + 16 * g);
            const v2u ht = *(LAS v2u*)(L + LB_H + (n & 1) * 128 + 64 + 8 * g);
            proj576_run<1, true>(ws, bid, wave, lane, wf, wt, hf, ht);
        }
        STAMP(11);
    }
}

constexpr int WS_DBG2 = 9 << 20;
__global__ void mark_kernel(char* wsp) { u64 t; asm volatile("s_memrealtime %0\n\ts_waitcnt lgkmcnt(0)" : "=s"(t) :: "memory"); *(GAS u64*)(wsp + WS_DBG2 - 512) = t; }
template <int LDS, int NTH, int VG>
__global__ void __launch_bounds__(NTH) x_kernel(char* wsp) {
    __shared__ char sm[LDS];
    u64 t; asm volatile("s_memrealtime %0\n\ts_waitcnt lgkmcnt(0)" : "=s"(t) :: "memory");
    if (LDS > 16) { sm[threadIdx.x] = 1; int vv = sm[(threadIdx.x * 7) % LDS]; asm volatile("" :: "v"(vv)); }
    if (VG) asm volatile("v_mov_b32 v143, 0" ::: "v143");
    if (threadIdx.x == 0) ((GAS u64*)(wsp + WS_DBG2))[blockIdx.x] = t;
}
__global__ void spin_kernel(char* wsp, int G) {
    u64 t0min = ~0ull;
    for (int i = 0; i < G; ++i) { u64 x = ((GAS u64*)(wsp + WS_DBG2))[i]; t0min = x < t0min ? x : t0min; }
    u64 span = t0min - *(GAS u64*)(wsp + WS_DBG2 - 512);
    if (span > 10000) span = 10000;
    u64 t, t1; asm volatile("s_memrealtime %0\n\ts_waitcnt lgkmcnt(0)" : "=s"(t) :: "memory");
    do { asm volatile("s_memrealtime %0\n\ts_waitcnt lgkmcnt(0)" : "=s"(t1) :: "memory"); } while (t1 < t + span * 100);
}
extern "C" void kernel_launch(void* const* d_in, const int* in_sizes, int n_in, void* d_out, int out_size, void* d_ws, size_t ws_size, hipStream_t stream) {
    mark_kernel<<<1, 64, 0, stream>>>((char*)d_ws);
    x_kernel<65536, 512, 0><<<256, 512, 0, stream>>>((char*)d_ws);
    fused_decode_kernel<<<256, NT, 0, stream>>>((const u16*)d_in[0], (const u16*)d_in[1], (const u16*)d_in[2], (const u16*)d_in[3], (const u16*)d_in[4],
                                                (const u16*)d_in[5], (const u16*)d_in[7], (const u16*)d_in[8], (const u16*)d_in[9], (const u16*)d_in[10],
                                                (const u16*)d_in[11], (u16*)d_out, (char*)d_ws);
    spin_kernel<<<1, 1, 0, stream>>>((char*)d_ws, 256);
}
```

```cpp
#define DBG_STAMP
#define STAMP_MASK 0x8001
#include <hip/hip_runtime.h>
#include <hip/hip_bf16.h>
#include <stdint.h>

typedef unsigned short u16;
typedef unsigned long long u64;
typedef unsigned v4u __attribute__((ext_vector_type(4)));
typedef unsigned v2u __attribute__((ext_vector_type(2)));
typedef int v4i __attribute__((ext_vector_type(4)));
typedef float f32x4 __attribute__((ext_vector_type(4)));
typedef float f32x2 __attribute__((ext_vector_type(2)));
typedef short s16x4 __attribute__((ext_vector_type(4)));
typedef __bf16 bf16x8 __attribute__((ext_vector_type(8)));
typedef __bf16 bf16x2 __attribute__((ext_vector_type(2)));
#define GAS __attribute__((address_space(1)))
#define LAS __attribute__((address_space(3)))
#define DI __device__ __forceinline__
typedef __amdgpu_buffer_rsrc_t rsrc_t;

constexpr int NT = 512;
constexpr unsigned SENT = 0xFFFFFFFFu;
constexpr int SC1 = 16;
constexpr unsigned SPIN_CAP = 1u << 17;
constexpr u64 TAG_STEP = 0x9E3779B97F4A7C14ull;

constexpr int WS_E = 0;
constexpr int WS_P1 = 4096;
constexpr int WS_R3 = WS_P1 + 16384;
constexpr int WS_C1 = WS_R3 + 16384;
constexpr int WS_H1 = WS_C1 + 16384;
constexpr int WS_H4 = WS_H1 + 128 * 1024;
constexpr int WS_QA = WS_H4 + 128 * 1024;
constexpr int WS_M1 = WS_QA + 65536;
constexpr int WS_ML = WS_M1 + 128 * 1152;
constexpr int WS_H3 = WS_ML + 16384;
constexpr int WS_H5 = WS_H3 + 64 * 32768;
constexpr int WS_END = WS_H5 + 128 * 4608;
constexpr unsigned NOWHERE = 0x7FFFFF00u;
constexpr int WS_DBG = 8 << 20;

constexpr int LA_WQ = 0, LA_WUK = 36864, LA_W1 = 69632, LA_WUV = 111104, LA_QF = 143872, LA_CMB = LA_QF, LA_COL = 147968, LA_TAG = 148096, LA_X = 148224, LA_RED = 148992;
constexpr int LB_CKV = 0, LB_W = 0, LB_Q = 131072, LB_P = 147456, LB_MX = 151552, LB_L = 152064, LB_H = 152576, LB_TAG = 153088, LB_CNT = 153104, LB_ZERO = 153216;
constexpr int LDS_BYTES = 153600;
constexpr int CODE_LINES = 154;

#define STAMP(i) do { if (!((STAMP_MASK >> (i)) & 1)) break; __builtin_amdgcn_sched_barrier(0); { u64 t_; asm volatile("s_memrealtime %0\n\ts_waitcnt lgkmcnt(0)" : "=s"(t_) :: "memory"); asm volatile("v_writelane_b32 %0, %1, %2" : "+v"(stampv) : "s"((unsigned)t_), "i"(i)); } __builtin_amdgcn_sched_barrier(0); } while (0)
#define STAMPE(i) STAMP(i)
#define STAMPD(i) do {} while (0)

DI v4i mk_srd(const void* p, unsigned bytes) {
    const u64 a = (u64)p;
    return (v4i){(int)(unsigned)a, (int)((unsigned)(a >> 32) & 0xFFFFu), (int)bytes, 0x00020000};
}
DI void dma16(v4i srd, unsigned voff, unsigned soff, unsigned lds) {
    asm volatile("s_mov_b32 m0, %0\n\ts_nop 0\n\tbuffer_load_dwordx4 %1, %2, %3 offen lds" :: "s"(lds), "v"(voff), "s"(srd), "s"(soff) : "memory");
}
#define W_NT 0
DI void dma16w(v4i srd, unsigned voff, unsigned soff, unsigned lds) {
    if (W_NT) asm volatile("s_mov_b32 m0, %0\n\ts_nop 0\n\tbuffer_load_dwordx4 %1, %2, %3 offen nt lds" :: "s"(lds), "v"(voff), "s"(srd), "s"(soff) : "memory");
    else dma16(srd, voff, soff, lds);
}
DI void touch(v4i srd, unsigned voff) { asm volatile("buffer_load_dword v255, %0, %1, 0 offen" :: "v"(voff), "s"(srd) : "memory", "v255"); }
DI u64 now() { u64 t; asm volatile("s_memrealtime %0\n\ts_waitcnt lgkmcnt(0)" : "=s"(t) :: "memory"); return t; }
DI void wait_until(u64 t) { while (now() < t) __builtin_amdgcn_s_sleep(2); }
DI v4u ald128(v4i srd, unsigned voff) { v4u r; asm volatile("buffer_load_dwordx4 %0, %1, %2, 0 offen sc1" : "=v"(r) : "v"(voff), "s"(srd) : "memory"); return r; }
DI v4u ald128p(v4i srd, unsigned voff) { v4u r; asm volatile("buffer_load_dwordx4 %0, %1, %2, 0 offen" : "=v"(r) : "v"(voff), "s"(srd) : "memory"); return r; }
DI v2u ald64p(v4i srd, unsigned voff) { v2u r; asm volatile("buffer_load_dwordx2 %0, %1, %2, 0 offen" : "=v"(r) : "v"(voff), "s"(srd) : "memory"); return r; }
DI void ald_e(v2u& r, v4i srd) { asm volatile("buffer_load_dwordx2 %0, off, %1, 0 sc1" : "+v"(r) : "s"(srd) : "memory"); }
DI void ald64_into(v2u& r, v4i srd, unsigned voff) { asm volatile("buffer_load_dwordx2 %0, %1, %2, 0 offen sc1" : "+v"(r) : "v"(voff), "s"(srd) : "memory"); }
#define WAIT_VM(N, ...) asm volatile("s_waitcnt vmcnt(" #N ")" : __VA_ARGS__ :: "memory")
DI v2u ald64(v4i srd, unsigned voff) { v2u r; asm volatile("buffer_load_dwordx2 %0, %1, %2, 0 offen sc1" : "=v"(r) : "v"(voff), "s"(srd) : "memory"); return r; }
DI bool flags_ok(v2u f, v2u tag) { return __all((f.x == tag.x) & (f.y == tag.y)); }
DI bool flags_ge(v2u f, v2u tag, unsigned stage) { return __all(((f.x | 3u) == (tag.x | 3u)) & ((f.x & stage) == stage) & (f.y == tag.y)); }
DI v2u mk_tag(v2u e) { const u64 t = (((u64)e.y << 32) | e.x) + TAG_STEP; return (v2u){(unsigned)t & ~3u, (unsigned)(t >> 32)}; }
template <class T> DI void pin(T& v) { asm volatile("" : "+v"(v)); }
DI void fence_sched() { __builtin_amdgcn_sched_barrier(0); }
template <int N> DI void wait_vm() { asm volatile("s_waitcnt vmcnt(%0)" :: "i"(N) : "memory"); }
DI void barrier_lds() { asm volatile("s_waitcnt lgkmcnt(0)\n\ts_barrier" ::: "memory"); }
DI f32x4 mfma32(v4u a, v4u b, f32x4 c) { return __builtin_amdgcn_mfma_f32_16x16x32_bf16(__builtin_bit_cast(bf16x8, a), __builtin_bit_cast(bf16x8, b), c, 0, 0, 0); }
DI f32x4 mfma16(v2u a, v2u b, f32x4 c) { return mfma32((v4u){a.x, a.y, 0u, 0u}, (v4u){b.x, b.y, 0u, 0u}, c); }
DI v2u lds_tr(LAS char* L, unsigned off) { return __builtin_bit_cast(v2u, __builtin_amdgcn_ds_read_tr16_b64_v4i16((LAS s16x4*)(L + off))); }
DI v4u lds_tr2(LAS char* L, unsigned o0, unsigned o1) { const v2u a = lds_tr(L, o0), b = lds_tr(L, o1); return (v4u){a.x, a.y, b.x, b.y}; }
DI v4u lds128(LAS char* L, unsigned off) { return *(LAS v4u*)(L + off); }
DI unsigned pk(float lo, float hi) { return __builtin_bit_cast(unsigned, __builtin_convertvector((f32x2){lo, hi}, bf16x2)); }
DI unsigned nos(unsigned v) { return v < 0xFFFFFFFEu ? v : 0xFFFFFFFEu; }
DI float bflo(unsigned v) { return __uint_as_float(v << 16); }
DI float bfhi(unsigned v) { return __uint_as_float(v & 0xFFFF0000u); }
DI float bfr(float f) { return bflo(pk(f, f)); }
template <int CTRL> DI float dpp(float x) { return __builtin_bit_cast(float, __builtin_amdgcn_mov_dpp(__builtin_bit_cast(int, x), CTRL, 0xf, 0xf, true)); }
DI float fmx(float a, float b) { return __builtin_fmaxf(a, b); }
constexpr int XOR1 = 0xB1, XOR2 = 0x4E, ROR4 = 0x124, ROR8 = 0x128;
DI float x16max(float x) { auto s = __builtin_amdgcn_permlane16_swap(__float_as_uint(x), __float_as_uint(x), false, false); return fmx(__uint_as_float(s[0]), __uint_as_float(s[1])); }
DI float x32max(float x) { auto s = __builtin_amdgcn_permlane32_swap(__float_as_uint(x), __float_as_uint(x), false, false); return fmx(__uint_as_float(s[0]), __uint_as_float(s[1])); }
DI float x16sum(float x) { auto s = __builtin_amdgcn_permlane16_swap(__float_as_uint(x), __float_as_uint(x), false, false); return __uint_as_float(s[0]) + __uint_as_float(s[1]); }
DI float x32sum(float x) { auto s = __builtin_amdgcn_permlane32_swap(__float_as_uint(x), __float_as_uint(x), false, false); return __uint_as_float(s[0]) + __uint_as_float(s[1]); }
DI unsigned umax2(unsigned a, unsigned b) { return a > b ? a : b; }
DI unsigned umax4(v4u v) { return umax2(umax2(v.x, v.y), umax2(v.z, v.w)); }
DI void st128(rsrc_t rs, v4u v, unsigned off) { __builtin_amdgcn_raw_buffer_store_b128(v, rs, off, 0, SC1); }
DI void st64(rsrc_t rs, v2u v, unsigned off) { __builtin_amdgcn_raw_buffer_store_b64(v, rs, off, 0, SC1); }
DI v4u ld128(rsrc_t rs, unsigned off) { return __builtin_amdgcn_raw_buffer_load_b128(rs, off, 0, SC1); }
DI v2u ld64(rsrc_t rs, unsigned off) { return __builtin_amdgcn_raw_buffer_load_b64(rs, off, 0, SC1); }
DI unsigned ld32(rsrc_t rs, unsigned off) { return __builtin_amdgcn_raw_buffer_load_b32(rs, off, 0, SC1); }

template <int SLP, unsigned STAGE = 0> DI void wait_flags(rsrc_t rs, unsigned off, v2u tag) {
#pragma unroll 1
    for (unsigned spins = 0; spins < SPIN_CAP; ++spins) {
        asm volatile("" ::: "memory");
        const v2u f = ld64(rs, off);
        if (flags_ge(f, tag, STAGE)) break;
        if (SLP) __builtin_amdgcn_s_sleep(SLP);
    }
    asm volatile("" ::: "memory");
}
template <int N> DI bool landed(const v4u (&r)[N]) {
    unsigned m = 0;
#pragma unroll
    for (int i = 0; i < N; ++i) m = umax2(m, umax4(r[i]));
    return !__any(m == SENT);
}
template <int N, int SLP = 0> DI void regather(rsrc_t rs, const unsigned (&off)[N], v4u (&r)[N]) {
#pragma unroll 1
    for (unsigned spins = 0; spins < SPIN_CAP; ++spins) {
        if (SLP) __builtin_amdgcn_s_sleep(SLP);
        asm volatile("" ::: "memory");
#pragma unroll
        for (int i = 0; i < N; ++i) {
            const bool miss = umax4(r[i]) == SENT;
            const v4u v = ld128(rs, miss ? off[i] : NOWHERE);
            r[i] = miss ? v : r[i];
        }
        if (landed<N>(r)) break;
    }
}

DI void lds_arrive(LAS char* L, int off, int lane) { if (lane == 0) __hip_atomic_fetch_add((LAS unsigned*)(L + off), 1u, __ATOMIC_RELAXED, __HIP_MEMORY_SCOPE_WORKGROUP); }
DI void lds_wait(LAS char* L, int off, unsigned count) {
#pragma unroll 1
    for (unsigned spins = 0; spins < (SPIN_CAP << 4); ++spins) {
        asm volatile("" ::: "memory");
        if (__builtin_amdgcn_readfirstlane(*(volatile LAS unsigned*)(L + off)) >= count) break;
    }
    asm volatile("" ::: "memory");
}
DI v4u tagged4(f32x4 c, v2u tag) {
    const unsigned p01 = pk(c[0], c[1]), p23 = pk(c[2], c[3]);
    return (v4u){__builtin_amdgcn_perm(p01, tag.x, 0x05040100), __builtin_amdgcn_perm(p01, tag.x, 0x07060302),
                 __builtin_amdgcn_perm(p23, tag.y, 0x05040100), __builtin_amdgcn_perm(p23, tag.y, 0x07060302)};
}
DI unsigned tag_miss(v4u t, v2u tag) { return (__builtin_amdgcn_perm(t.y, t.x, 0x05040100) ^ tag.x) | (__builtin_amdgcn_perm(t.w, t.z, 0x05040100) ^ tag.y); }
DI float gate_of(const v4u (&xv)[2], const v4u (&gv)[2]) {
    float s = 0.f;
#pragma unroll
    for (int i = 0; i < 2; ++i)
#pragma unroll
        for (int k = 0; k < 4; ++k) { const unsigned pr = pk(bflo(xv[i][k]) * bflo(gv[i][k]), bfhi(xv[i][k]) * bfhi(gv[i][k])); s += bflo(pr) + bfhi(pr); }
    s += dpp<XOR1>(s); s += dpp<XOR2>(s); s += dpp<ROR4>(s); s += dpp<ROR8>(s); s = x32sum(x16sum(s));
    return bfr(1.f / (1.f + __expf(-bfr(s))));
}

#define A_DELAY 28
#define PF_DELAY 12
#define H1_DELAY 10
#define H3_DELAY 0
#define H5_DELAY 20
#define FA_AT 10
#define FAST_Q 1
#define Q_SLP 2
#define TR_EARLY 8
#define W_PF 0
#define R3_AT 2
#define W_DELAY 0
#define DRY 1
#define CL_FIRST 0
#define H4_DELAY 0
#define C1_GATE 0
#define APF 256
#define UK_PF 1
#define Q_DELAY 0
#define A_CLEAN 0
#define A_T 330
#define PF_T 105
#define CK_MIX 8
#define CK_BAR 0
#define OUT_AUX 0
#define CODE_PF 1
__global__ void __launch_bounds__(NT) __attribute__((amdgpu_waves_per_eu(2, 2)))
fused_decode_kernel(const u16* __restrict__ xp, const u16* __restrict__ ckv, const u16* __restrict__ wq, const u16* __restrict__ wuk,
                    const u16* __restrict__ wuv, const u16* __restrict__ wo, const u16* __restrict__ w1, const u16* __restrict__ b1,
                    const u16* __restrict__ w2, const u16* __restrict__ b2, const u16* __restrict__ gw, u16* __restrict__ out, char* wsp) {
    __shared__ __attribute__((aligned(1024))) char smem[LDS_BYTES];
    LAS char* L = (LAS char*)smem;
    const unsigned lds0 = (unsigned)(size_t)L;
    unsigned stampv = 0;
    STAMP(0);
    const u64 t0 = now();
    const int bid = blockIdx.x, tid = threadIdx.x, lane = tid & 63;
    const int xcd = bid & 7, slot = bid >> 3;
    if (CODE_PF) {
        u64 pc0; asm volatile("s_getpc_b64 %0" : "=s"(pc0));
        if (slot + 32 * tid < CODE_LINES)
            asm volatile("global_load_dword v255, %0, %1" :: "v"((slot + 32 * tid) * 128), "s"(pc0 & ~127ull) : "memory", "v255");
    }
    asm volatile("" :: "s"(xp), "s"(ckv), "s"(wq), "s"(wuk), "s"(wuv), "s"(wo), "s"(w1), "s"(b1), "s"(w2), "s"(b2), "s"(gw), "s"(out), "s"(wsp));
    const int wave = __builtin_amdgcn_readfirstlane(tid >> 6);
    const int g = lane >> 4, n = lane & 15, q = (lane >> 2) & 3, p = lane & 3;
    const rsrc_t ws = __builtin_amdgcn_make_buffer_rsrc(wsp, 0, WS_END, 0x00020000);
    const v4i wsd = mk_srd(wsp, WS_END);
    const v4u CLEAN = {SENT, SENT, SENT, SENT};

    if (__builtin_expect(slot < 16, 1)) {
        const int a = xcd * 16 + slot, h = a >> 2, j = a & 3;
        asm volatile("s_nop 4" ::: "memory");
        const unsigned v_t = g * 8192 + 16 * (n ^ ((4 * ((wave >> 1) & 1) + g) << 1));
        {
            const v4i srd = mk_srd(wq + (size_t)j * 144 * 4096 + h * 128, 143 * 8192 + 256);
#pragma unroll
            for (int i = 0; i < 5; ++i)
                if (i < 4 || wave < 4) dma16w(srd, v_t, (wave + 8 * i) * 32768, lds0 + LA_WQ + (wave + 8 * i) * 1024);
        }
        v2u e = {0u, 0u};
        if (wave == 7) {
            ald_e(e, wsd);
            if (lane < 36) dma16(mk_srd(xp, 2304), (lane >= 18 ? 1152 - 288 : 0) + j * 288 + lane * 16, 0, lds0 + LA_X);
            if (lane < 32) st128(ws, CLEAN, WS_QA + g * 32768 + h * 1024 + j * 256 + n * 16);
        }
        const v4i srd_uk = mk_srd(wuk + (size_t)j * 128 * 4096 + h * 128, 127 * 8192 + 256);
        unsigned v_uk[4], a_uk[4];
#pragma unroll
        for (int i = 0; i < 4; ++i) {
            v_uk[i] = g * 8192 + 16 * (n ^ (4 * i + g)); a_uk[i] = LA_WUK + (16 * wave + n) * 256 + 16 * ((4 * i + g) ^ n);
            pin(v_uk[i]); pin(a_uk[i]);
        }
#define UK_ROWS() do { _Pragma("unroll") for (int i = 0; i < 4; ++i) dma16w(srd_uk, v_uk[i], (16 * wave + 4 * i) * 8192, lds0 + LA_WUK + (16 * wave + 4 * i) * 256); } while (0)
        if (!UK_PF) { asm volatile("s_barrier" ::: "memory"); UK_ROWS(); }
        unsigned a_x = LA_X + (n & 1) * 288 + 16 * g, a_xt = LA_X + (n & 1) * 288 + 256 + 8 * g;
        unsigned a_q = (8 * g + q) * 256 + 16 * ((2 * wave + (p >> 1)) ^ ((q + 4 * (g & 1)) << 1)) + 8 * (p & 1);
        unsigned a_qt = LA_WQ + (128 + 4 * g + q) * 256 + 16 * ((2 * wave + (p >> 1)) ^ ((q + 4 * (g >> 1)) << 1)) + 8 * (p & 1);
        unsigned o_h1 = n < 2 ? WS_H1 + a * 1024 + wave * 128 + n * 64 + g * 16 : NOWHERE;
        unsigned o_g1 = WS_H1 + (4 * h + ((n >> 1) & 3)) * 1024 + (wave & 1) * 512 + (g >> 1) * 128 + (n & 1) * 64 + (g & 1) * 32;
        unsigned o_qa = n < 2 ? WS_QA + n * 32768 + h * 1024 + (128 * j + 16 * wave + 4 * g) * 2 : NOWHERE;
        unsigned a_l16 = LA_QF + lane * 16;
        pin(a_x); pin(a_xt); pin(a_q); pin(a_qt); pin(o_h1); pin(o_g1); pin(o_qa); pin(a_l16);
        if (UK_PF) WAIT_VM(0, "+v"(e)); else WAIT_VM(4, "+v"(e));
        if (tid == 448) *(LAS v2u*)(L + LA_TAG) = mk_tag(e);
        STAMP(1);
        barrier_lds();
        const v2u tag = *(LAS v2u*)(L + LA_TAG);
        v4u xf[4];
#pragma unroll
        for (int s = 0; s < 4; ++s) xf[s] = lds128(L, a_x + 64 * s);
        const v2u xt = *(LAS v2u*)(L + a_xt);
        {
            v4u wf[4];
#pragma unroll
            for (int s = 0; s < 4; ++s) wf[s] = lds_tr2(L, LA_WQ + a_q + s * 8192, LA_WQ + a_q + s * 8192 + 1024);
            const v2u wt = lds_tr(L, a_qt);
            fence_sched();
            f32x4 c = {0.f, 0.f, 0.f, 0.f};
#pragma unroll
            for (int s = 0; s < 4; ++s) c = mfma32(wf[s], xf[s], c);
            c = mfma16(wt, xt, c);
            st128(ws, tagged4(c, tag), o_h1);
        }
        if (wave >= 6) {
            if (tid == 448) st64(ws, tag, WS_P1 + a * 128);
            if (tid < 384 + 72) st128(ws, CLEAN, WS_M1 + a * 1152 + (tid - 384) * 16);
        }
        STAMP(2);
        if (wave < 2) {
            if (!UK_PF) wait_vm<0>();
            if (H1_DELAY) __builtin_amdgcn_s_sleep(H1_DELAY);
            v4u t[4];
#pragma unroll
            for (int s = 0; s < 4; ++s) asm volatile("buffer_load_dwordx4 %0, %1, %2, 0 offen offset:%3 sc1" : "=v"(t[s]) : "v"(o_g1), "s"(wsd), "i"((s >> 1) * 256 + (s & 1) * 16) : "memory");
            if (UK_PF) {
                asm volatile("s_barrier" ::: "memory");
                UK_ROWS();
                WAIT_VM(4, "+v"(t[0]), "+v"(t[1]), "+v"(t[2]), "+v"(t[3]));
            } else WAIT_VM(0, "+v"(t[0]), "+v"(t[1]), "+v"(t[2]), "+v"(t[3]));
            STAMP(3);
            if (__builtin_expect(__any((tag_miss(t[0], tag) | tag_miss(t[1], tag) | tag_miss(t[2], tag) | tag_miss(t[3], tag)) != 0), 0)) {
#pragma unroll 1
                for (unsigned spins = 0; spins < SPIN_CAP; ++spins) {
                    asm volatile("" ::: "memory");
                    unsigned bad = 0;
#pragma unroll
                    for (int s = 0; s < 4; ++s) {
                        const bool miss = tag_miss(t[s], tag) != 0;
                        const v4u v = ld128(ws, miss ? o_g1 + (s >> 1) * 256 + (s & 1) * 16 : NOWHERE);
                        t[s] = miss ? v : t[s];
                    }
#pragma unroll
                    for (int s = 0; s < 4; ++s) bad |= tag_miss(t[s], tag);
                    if (!__any(bad != 0)) break;
                }
            }
#pragma unroll
            for (int s = 0; s < 2; ++s)
                *(LAS v4u*)(L + a_l16 + (2 * wave + s) * 1024) =
                    (v4u){__builtin_amdgcn_perm(t[2 * s].y, t[2 * s].x, 0x07060302), __builtin_amdgcn_perm(t[2 * s].w, t[2 * s].z, 0x07060302),
                          __builtin_amdgcn_perm(t[2 * s + 1].y, t[2 * s + 1].x, 0x07060302), __builtin_amdgcn_perm(t[2 * s + 1].w, t[2 * s + 1].z, 0x07060302)};
        } else if (UK_PF) {
            asm volatile("s_barrier" ::: "memory");
            UK_ROWS();
        }
        wait_vm<0>();
        v4u uf[4];
#pragma unroll
        for (int s = 0; s < 4; ++s) uf[s] = lds128(L, a_uk[s]);
        barrier_lds();
        STAMP(4);
        {
            v4u qf[4];
#pragma unroll
            for (int s = 0; s < 4; ++s) qf[s] = lds128(L, a_l16 + s * 1024);
            fence_sched();
            if (tid == 448) st64(ws, (v2u){tag.x | 1u, tag.y}, WS_P1 + a * 128);
            f32x4 c = {0.f, 0.f, 0.f, 0.f};
#pragma unroll
            for (int s = 0; s < 4; ++s) c = mfma32(uf[s], qf[s], c);
            constexpr float SC = 0.08838834764831845f * 1.4426950408889634f;
#pragma unroll
            for (int r = 0; r < 4; ++r) { c[r] += dpp<XOR2>(c[r]); c[r] += dpp<ROR4>(c[r]); c[r] *= SC; }
            st64(ws, (v2u){nos(pk(c[0], c[1])), nos(pk(c[2], c[3]))}, o_qa);
        }
        STAMP(5);
        if (A_CLEAN) {
            const unsigned o = WS_H3 + (tid >> 4) * 32768 + h * 1024 + j * 256 + n * 16;
            st128(ws, CLEAN, o); st128(ws, CLEAN, o + 32 * 32768);
        }
        wait_until(t0 + A_T);
        if (A_CLEAN) {
            wait_vm<0>();
            asm volatile("s_barrier" ::: "memory");
            if (tid == 448) st64(ws, (v2u){tag.x | 3u, tag.y}, WS_P1 + a * 128);
        }
        {
            const v4i srd = mk_srd(w1 + (size_t)j * 144 * 4608 + h * 144, 143 * 9216 + 288);
            const unsigned voff = (lane / 18) * 9216 + (lane % 18) * 16;
            if (lane < 54) {
#pragma unroll
                for (int i = 0; i < 6; ++i) dma16(srd, voff, (wave + 8 * i) * 27648, lds0 + LA_W1 + (wave + 8 * i) * 864);
            }
        }
        asm volatile("s_barrier" ::: "memory");
        {
            const v4i srd = mk_srd(wuv + (size_t)j * 128 * 4096 + h * 128, 127 * 8192 + 256);
#pragma unroll
            for (int i = 0; i < 4; ++i) dma16w(srd, v_t, (wave + 8 * i) * 32768, lds0 + LA_WUV + (wave + 8 * i) * 1024);
        }
        STAMP(6);
        wait_vm<4>();
        barrier_lds();
        STAMP(7);
        {
            const unsigned a0 = LA_W1 + (8 * g + q) * 288 + 8 * p + 32 * wave, at = LA_W1 + (128 + 4 * g + q) * 288 + 8 * p + 32 * wave;
#pragma unroll
            for (int t = 0; t < 2; ++t) {
                if (t == 0 || wave == 2) {
                    f32x4 c = {0.f, 0.f, 0.f, 0.f};
#pragma unroll
                    for (int s = 0; s < 4; ++s) c = mfma32(lds_tr2(L, a0 + t * 192 + s * 9216, a0 + t * 192 + s * 9216 + 1152), xf[s], c);
                    c = mfma16(lds_tr(L, at + t * 192), xt, c);
                    const v4u v = {nos(__float_as_uint(c[0])), nos(__float_as_uint(c[1])), nos(__float_as_uint(c[2])), nos(__float_as_uint(c[3]))};
                    if (n < 2) st128(ws, v, WS_M1 + a * 1152 + n * 576 + (16 * wave + 96 * t + 4 * g) * 4);
                }
            }
        }
        if (APF) {
            const int bi = (slot & 1) * 64 + xcd * 8 + (slot >> 1);
            asm volatile("s_barrier" ::: "memory");
            touch(mk_srd(w2 + (size_t)bi * 36 * 576, 36 * 1152), tid < (APF < 324 ? APF : 324) ? tid * 128 : NOWHERE);
            touch(mk_srd(wo + (size_t)bi * 32 * 576, 32 * 1152), tid < APF - 324 ? (287 - tid) * 128 : NOWHERE);
        }
        STAMP(8);
        float obias = 0.f;
        if (bid < 36 && wave == 7) {
            const rsrc_t rx = __builtin_amdgcn_make_buffer_rsrc((void*)(xp + (bid / 18) * 576), 0, 1152, 0x00020000), rg = __builtin_amdgcn_make_buffer_rsrc((void*)gw, 0, 1152, 0x00020000);
            const v4u xv[2] = {__builtin_amdgcn_raw_buffer_load_b128(rx, lane * 16, 0, 0), __builtin_amdgcn_raw_buffer_load_b128(rx, lane * 16 + 1024, 0, 0)};
            const v4u gv[2] = {__builtin_amdgcn_raw_buffer_load_b128(rg, lane * 16, 0, 0), __builtin_amdgcn_raw_buffer_load_b128(rg, lane * 16 + 1024, 0, 0)};
            obias = bfr(1.f - gate_of(xv, gv)) * bflo(((const GAS u16*)b2)[bid * 32 + (lane & 31) - (bid / 18) * 576]);
        }
        const int b = wave >> 2, hf = lane >> 5, li = lane & 31, k0 = 32 * b + 8 * (wave & 3) + 4 * hf, col = 4 * (wave & 3) + 2 * hf + b;
        unsigned o_h3 = WS_H3 + (k0 + 2 * (g & 1)) * 32768 + h * 1024 + j * 256 + n * 16, o_ml = WS_ML + (k0 + p) * 256 + h * 8;
        unsigned a_cw = LA_CMB + col * 256 + 16 * (n ^ col) + 8 * (g & 1), a_col = LA_COL + 8 * col, a_colr = LA_COL + 8 * n;
        unsigned a_cr[4], o5 = WS_H5 + ((lane >> 3) + 8 * wave) * 4608 + bid * 128 + (lane & 7) * 16;
#pragma unroll
        for (int s = 0; s < 4; ++s) { a_cr[s] = LA_CMB + n * 256 + 16 * ((4 * s + g) ^ n); pin(a_cr[s]); }
        unsigned a_rw = LA_RED + ((lane & 7) * 4 + 2 * hf + (g & 1)) * 32 + wave * 4, a_rr = LA_RED + li * 32;
        pin(o_h3); pin(o_ml); pin(a_cw); pin(a_col); pin(a_colr); pin(o5); pin(a_rw); pin(a_rr);
        if (wave == 0 || (wave == 1 && bid < 36)) wait_flags<0>(ws, WS_R3 + (((h >> 4) ^ wave) * 64 + lane) * 128, tag);
        if (H3_DELAY) __builtin_amdgcn_s_sleep(H3_DELAY);
        if (APF) wait_vm<3>(); else wait_vm<1>();
        asm volatile("s_barrier" ::: "memory");
        STAMP(9);
        v4u r[2]; v2u ml;
        asm volatile("buffer_load_dwordx4 %0, %1, %2, 0 offen sc1" : "=v"(r[0]) : "v"(o_h3), "s"(wsd) : "memory");
        asm volatile("buffer_load_dwordx4 %0, %1, %2, %3 offen sc1" : "=v"(r[1]) : "v"(o_h3), "s"(wsd), "s"(32768) : "memory");
        asm volatile("buffer_load_dwordx2 %0, %1, %2, 0 offen sc1" : "=v"(ml) : "v"(o_ml), "s"(wsd) : "memory");
        v4u vf[4];
#pragma unroll
        for (int s = 0; s < 4; ++s) vf[s] = lds_tr2(L, LA_WUV + a_q + s * 8192, LA_WUV + a_q + s * 8192 + 1024);
        WAIT_VM(0, "+v"(r[0]), "+v"(r[1]), "+v"(ml));
        STAMP(13);
        if (__builtin_expect(__any(umax2(umax2(umax4(r[0]), umax4(r[1])), umax2(ml.x, ml.y)) == SENT), 0)) {
#pragma unroll 1
            for (unsigned spins = 0; spins < SPIN_CAP; ++spins) {
                asm volatile("" ::: "memory");
                const bool m0 = umax4(r[0]) == SENT, m1 = umax4(r[1]) == SENT, m2 = umax2(ml.x, ml.y) == SENT;
                const v4u v0 = ld128(ws, m0 ? o_h3 : NOWHERE), v1 = ld128(ws, m1 ? o_h3 + 32768 : NOWHERE);
                const v2u v2 = ld64(ws, m2 ? o_ml : NOWHERE);
                r[0] = m0 ? v0 : r[0]; r[1] = m1 ? v1 : r[1]; ml = m2 ? v2 : ml;
                if (!__any(umax2(umax2(umax4(r[0]), umax4(r[1])), umax2(ml.x, ml.y)) == SENT)) break;
            }
        }
        STAMP(10);
        {
            float mx = fmx(__uint_as_float(ml.x), dpp<XOR1>(__uint_as_float(ml.x))); mx = fmx(mx, dpp<XOR2>(mx));
            const float w = __builtin_amdgcn_exp2f(__uint_as_float(ml.x) - mx);
            const int wi = __float_as_int(w);
            const float w0 = __int_as_float(__builtin_amdgcn_update_dpp(__builtin_amdgcn_mov_dpp(wi, 0x00, 0xf, 0xf, true), wi, 0xAA, 0xa, 0xf, false));
            const float w1 = __int_as_float(__builtin_amdgcn_update_dpp(__builtin_amdgcn_mov_dpp(wi, 0x55, 0xf, 0xf, true), wi, 0xFF, 0xa, 0xf, false));
            float acc[4];
#pragma unroll
            for (int k = 0; k < 2; ++k) {
                const float a0 = fmaf(w1, bflo(r[1][k]), w0 * bflo(r[0][k])), a1 = fmaf(w1, bfhi(r[1][k]), w0 * bfhi(r[0][k]));
                const float b0 = fmaf(w1, bflo(r[1][k + 2]), w0 * bflo(r[0][k + 2])), b1 = fmaf(w1, bfhi(r[1][k + 2]), w0 * bfhi(r[0][k + 2]));
                auto s0 = __builtin_amdgcn_permlane16_swap(__float_as_uint(a0), __float_as_uint(b0), false, false);
                auto s1 = __builtin_amdgcn_permlane16_swap(__float_as_uint(a1), __float_as_uint(b1), false, false);
                acc[2 * k] = __uint_as_float(s0[0]) + __uint_as_float(s0[1]); acc[2 * k + 1] = __uint_as_float(s1[0]) + __uint_as_float(s1[1]);
            }
            *(LAS v2u*)(L + a_cw) = (v2u){pk(acc[0], acc[1]), pk(acc[2], acc[3])};
            float ls = w * __uint_as_float(ml.y); ls += dpp<XOR1>(ls); ls += dpp<XOR2>(ls);
            *(LAS f32x2*)(L + a_col) = (f32x2){mx, ls};
        }
        barrier_lds();
        STAMP(11);
        {
            const f32x2 cm = *(LAS f32x2*)(L + a_colr);
            v4u cf[4];
#pragma unroll
            for (int s = 0; s < 4; ++s) cf[s] = lds128(L, a_cr[s]);
            fence_sched();
            f32x4 c = {0.f, 0.f, 0.f, 0.f};
#pragma unroll
            for (int s = 0; s < 4; ++s) c = mfma32(vf[s], cf[s], c);
            float mx = fmx(cm[0], dpp<XOR2>(cm[0])); mx = fmx(mx, dpp<ROR4>(mx)); mx = fmx(mx, dpp<ROR8>(mx));
            const float wc = __builtin_amdgcn_exp2f(cm[0] - mx);
            float ls = wc * cm[1]; ls += dpp<XOR2>(ls); ls += dpp<ROR4>(ls); ls += dpp<ROR8>(ls);
            const float sc = wc * __builtin_amdgcn_rcpf(ls);
#pragma unroll
            for (int k = 0; k < 4; ++k) { c[k] *= sc; c[k] += dpp<XOR2>(c[k]); c[k] += dpp<ROR4>(c[k]); c[k] += dpp<ROR8>(c[k]); }
            st128(ws, tagged4(c, tag), o_h1 + (WS_H4 - WS_H1));
        }
        STAMP(12);
        if (bid < 36) {
            int npass = 2;
            asm volatile("" : "+s"(npass));
#pragma unroll 1
            for (int pass = DRY ? 0 : 1; pass < npass; ++pass) {
                v4u r5[2] = {{0u, 0u, 0u, 0u}, {0u, 0u, 0u, 0u}};
                if (pass) {
                    if (H5_DELAY) __builtin_amdgcn_s_sleep(H5_DELAY);
                    const unsigned off[2] = {o5, o5 + 64 * 4608};
                    r5[0] = ld128(ws, off[0]); r5[1] = ld128(ws, off[1]);
                    if (!landed<2>(r5)) regather<2>(ws, off, r5);
                    STAMP(14);
                }
                float t[4];
#pragma unroll
                for (int k = 0; k < 4; ++k) t[k] = __uint_as_float(r5[0][k]) + __uint_as_float(r5[1][k]);
                auto s0 = __builtin_amdgcn_permlane32_swap(__float_as_uint(t[0]), __float_as_uint(t[2]), false, false);
                auto s1 = __builtin_amdgcn_permlane32_swap(__float_as_uint(t[1]), __float_as_uint(t[3]), false, false);
                const float u0 = __uint_as_float(s0[0]) + __uint_as_float(s0[1]), u1 = __uint_as_float(s1[0]) + __uint_as_float(s1[1]);
                auto s2 = __builtin_amdgcn_permlane16_swap(__float_as_uint(u0), __float_as_uint(u1), false, false);
                float v = __uint_as_float(s2[0]) + __uint_as_float(s2[1]);
                v += dpp<ROR8>(v);
                *(LAS float*)(L + a_rw) = v;
                barrier_lds();
                if (wave == 7) {
                    const f32x4 x0 = *(LAS f32x4*)(L + a_rr), x1 = *(LAS f32x4*)(L + a_rr + 16);
                    const float res = (((x0[0] + x0[1]) + (x0[2] + x0[3])) + ((x1[0] + x1[1]) + (x1[2] + x1[3]))) + obias;
                    __builtin_amdgcn_raw_buffer_store_b16((u16)pk(res, res), __builtin_amdgcn_make_buffer_rsrc(out, 0, 2304, 0x00020000), pass && lane < 32 ? bid * 64 + lane * 2 : NOWHERE, 0, OUT_AUX);
                    st64(ws, tag, pass && bid == 0 && lane == 0 ? WS_E : NOWHERE);
                }
                if (!pass) barrier_lds();
            }
            STAMP(15);
        }
    } else {
        const int sb = slot - 16, chunk = xcd * 8 + (sb >> 1), hh = sb & 1, b = chunk >> 5, bi = hh * 64 + chunk;
        asm volatile("s_nop 4" ::: "memory");
        v2u e = {0u, 0u};
        if (wave == 0) ald_e(e, wsd);
        wait_until(t0 + PF_T);
        if (UK_PF == 1) {
            if (tid < 256) touch(mk_srd(wuk, 512 * 8192), (sb * 32 + (tid >> 3)) * 8192 + xcd * 1024 + (tid & 7) * 128);
            asm volatile("s_barrier" ::: "memory");
        }
        STAMPE(1);
        v2u fa = {0u, 0u};
        const v4i srd_c = mk_srd(ckv + (size_t)chunk * 128 * 512, 131072);
#define CKV_ROW(i) do { const int r = (((i) + wave) & 7) + 8 * (((i) >> 3) ^ hh); \
            dma16(srd_c, (16 * lane) ^ (16 * (2 * (r & 7) + (r >> 3))), (16 * wave + r) * 1024, lds0 + LB_CKV + (16 * wave + r) * 1024); } while (0)
#pragma unroll
        for (int i = 0; i < 8; ++i) CKV_ROW(i);
        fence_sched();
        unsigned o_c3 = WS_H3 + chunk * 32768 + hh * 16384 + tid * 16;
        unsigned o_c5 = tid < 288 ? WS_H5 + bi * 4608 + tid * 16 : tid < 296 ? WS_ML + chunk * 256 + hh * 128 + (tid - 288) * 16 : NOWHERE;
#define B_MARKS() do { if (!A_CLEAN) { st128(ws, CLEAN, o_c3); st128(ws, CLEAN, o_c3 + 8192); } st128(ws, CLEAN, o_c5); } while (0)
        if (tid < 2) *(LAS unsigned*)(L + LB_CNT + 4 * tid) = 0u;
        if (tid < 4) *(LAS unsigned*)(L + LB_ZERO + tid * 4) = 0u;
        const int fsw = 2 * (n & 7) + (n >> 3), pg = ((g & 1) << 1) | (g >> 1), rr = 4 * g + q, fr = 2 * (rr & 7) + (rr >> 3);
        unsigned a_k[4], a_qr[4], a_v[4][2];
#pragma unroll
        for (int s = 0; s < 4; ++s) {
            a_qr[s] = LB_Q + n * 1024 + 16 * ((4 * s + pg) ^ fsw); a_k[s] = a_qr[s] - LB_Q + LB_CKV + wave * 16384;
            a_v[s][0] = LB_CKV + rr * 1024 + 256 * (wave >> 1) + 16 * ((8 * (wave & 1) + 2 * s + (p >> 1)) ^ fr) + 8 * (p & 1); a_v[s][1] = a_v[s][0] + 65536;
            pin(a_qr[s]); pin(a_k[s]); pin(a_v[s][0]); pin(a_v[s][1]);
        }
        unsigned o_qa = WS_QA + b * 32768 + hh * 16384 + tid * 16, a_qw0 = LB_Q + wave * 1024 + 16 * (lane ^ (2 * wave)), a_qw1 = LB_Q + (wave + 8) * 1024 + 16 * (lane ^ (2 * wave + 1));
        unsigned a_mx = LB_MX + n * 32, a_mw = LB_MX + n * 32 + wave * 4, a_pw = LB_P + (wave >> 1) * 1024 + lane * 16 + (wave & 1) * 8, a_pr = LB_P + lane * 16;
        unsigned o_h3 = WS_H3 + chunk * 32768 + (hh * 16 + (n & 7)) * 1024 + wave * 128 + (n >> 3) * 64 + pg * 16;
        unsigned o_p1 = WS_P1 + (hh * 64 + lane) * 128, o_ml = g == 0 ? WS_ML + chunk * 256 + (hh * 16 + n) * 8 : NOWHERE, o_h3b = o_h3 + 8192;
        pin(o_ml); pin(o_h3b);
        pin(o_qa); pin(a_qw0); pin(a_qw1); pin(a_mx); pin(a_mw); pin(a_pw); pin(a_pr); pin(o_h3); pin(o_p1); pin(o_c3); pin(o_c5);
        const v4i srd_o = mk_srd(wo + (size_t)bi * 32 * 576, 32 * 1152), srd_2 = mk_srd(w2 + (size_t)bi * 36 * 576, 36 * 1152);
        unsigned v_w = (lane >> 3) * 1152 + 16 * ((lane & 7) ^ ((((lane >> 4) & 1) | ((wave & 1) << 1)) << 1));
        unsigned a_w = LB_W + g * 9216 + (wave >> 1) * 3072 + q * 128 + 16 * ((4 * (wave & 1) + (p >> 1)) ^ (((q >> 1) | ((g & 1) << 1)) << 1)) + 8 * (p & 1), a_w1 = a_w ^ 32;
        unsigned a_wt = LB_W + 8 * 9216 + (wave >> 1) * 3072 + q * 128 + 16 * ((4 * (wave & 1) + (p >> 1)) ^ ((q >> 1) << 1)) + 8 * (p & 1), a_wt1 = a_wt ^ 32;
        unsigned o_h5 = n < 12 ? WS_H5 + bi * 4608 + (n & 1) * 2304 + ((3 * (wave >> 1) + (n >> 2)) * 64 + (2 * (wave & 1) + ((n >> 1) & 1)) * 16 + 4 * g) * 4 : NOWHERE;
        unsigned a_h = LB_H + (n & 1) * 256 + 16 * g, a_ht = LB_H + (n & 1) * 256 + 128 + 8 * g, a_c[6];
#pragma unroll
        for (int i = 0; i < 6; ++i) { a_c[i] = (n >> 1) == i ? a_h : LB_ZERO; pin(a_c[i]); }
        unsigned o1 = WS_M1 + (bi & ~3) * 1152 + (wave & 1) * 576 + ((bi & 3) * 36 + (lane < 36 ? lane : 35)) * 4;
        unsigned o4 = WS_H4 + ((bi & ~3) + ((lane >> 3) & 3)) * 1024 + (2 * (bi & 3) + ((lane >> 2) & 1)) * 128 + (wave & 1) * 64 + (lane & 3) * 16;
        unsigned a_cx = LB_H + (wave & 1) * 256 + (lane & 7) * 8;
        pin(v_w); pin(a_w); pin(a_w1); pin(a_wt); pin(a_wt1); pin(o_h5); pin(a_h); pin(a_ht); pin(o1); pin(o4); pin(a_cx);
        v4u m1 = {0u, 0u, 0u, 0u}, xv[2] = {m1, m1}, gv[2] = {m1, m1};
        unsigned b1v = 0;
        if (wave >= 6) {
            const v4i sx = mk_srd(xp + (wave & 1) * 576, 1152), sg = mk_srd(gw, 1152);
#pragma unroll
            for (int k = 0; k < 2; ++k) {
                asm volatile("buffer_load_dwordx4 %0, %1, %2, 0 offen offset:%3" : "+v"(xv[k]) : "v"(lane * 16), "s"(sx), "i"(k * 1024) : "memory");
                asm volatile("buffer_load_dwordx4 %0, %1, %2, 0 offen offset:%3" : "+v"(gv[k]) : "v"(lane * 16), "s"(sg), "i"(k * 1024) : "memory");
            }
            asm volatile("buffer_load_ushort %0, %1, %2, 0 offen" : "+v"(b1v) : "v"(lane * 2), "s"(mk_srd(b1 + bi * 36, 72)) : "memory");
        }
        pin(m1);
        asm volatile("s_barrier" ::: "memory");
#pragma unroll
        for (int i = 8; i < 16; ++i) {
            if (FAST_Q && i == FA_AT && wave == 0) ald64_into(fa, wsd, o_p1);
            CKV_ROW(i);
        }
        STAMPE(2);
        asm volatile("s_waitcnt vmcnt(%7)" : "+v"(fa), "+v"(e), "+v"(xv[0]), "+v"(xv[1]), "+v"(gv[0]), "+v"(gv[1]), "+v"(b1v) : "i"(16 - FA_AT) : "memory");
        const v2u tag = mk_tag(e);
        if (wave == 0) {
            if (lane == 0) *(LAS v2u*)(L + LB_TAG) = tag;
            if (!(FAST_Q && flags_ge(fa, tag, 0))) wait_flags<0, 1>(ws, o_p1, tag);
        }
        if (Q_DELAY && wave == 0) __builtin_amdgcn_s_sleep(Q_DELAY);
        asm volatile("s_barrier" ::: "memory");
        STAMPE(3);
        v4u kf[16];
        {
            v4u r[2];
            const unsigned off[2] = {o_qa, o_qa + 8192};
            r[0] = ald128(wsd, off[0]); r[1] = ald128(wsd, off[1]);
            asm volatile("s_barrier" ::: "memory"); B_MARKS(); wait_vm<5>();
            STAMP(12);
#pragma unroll
            for (int s = 0; s < 16; ++s) kf[s] = lds128(L, a_k[s & 3] + 256 * (s >> 2));
            asm volatile("s_waitcnt vmcnt(%2)" : "+v"(r[0]), "+v"(r[1]) : "i"(3) : "memory");
            STAMP(13);
            if (__builtin_expect(!landed<2>(r), 0)) regather<2, Q_SLP>(ws, off, r);
            *(LAS v4u*)(L + a_qw0) = r[0]; *(LAS v4u*)(L + a_qw1) = r[1];
        }
        STAMP(4);
        barrier_lds();
        STAMP(5);
        fence_sched();
        if (A_CLEAN && wave == 0) ald64_into(fa, wsd, o_p1);
        if (W_PF && tid < W_PF)
            touch(mk_srd(w2 + (size_t)bi * 36 * 576, 36 * 1152), tid * 128);
        f32x4 sc = {0.f, 0.f, 0.f, 0.f};
        {
            v4u qf[16];
#pragma unroll
            for (int s = 0; s < 16; ++s) qf[s] = lds128(L, a_qr[s & 3] + 256 * (s >> 2));
            fence_sched();
#pragma unroll
            for (int s = 0; s < 16; ++s) sc = mfma32(kf[s], qf[s], sc);
        }
        *(LAS float*)(L + a_mw) = x32max(x16max(fmx(fmx(sc[0], sc[1]), fmx(sc[2], sc[3]))));
        if (R3_AT == 2) {
            if (W_PF && wave * 64 < W_PF) WAIT_VM(1, "+v"(fa)); else WAIT_VM(0, "+v"(fa));
            if (A_CLEAN && wave == 0 && !flags_ge(fa, tag, 3)) wait_flags<0, 3>(ws, o_p1, tag);
        }
        barrier_lds();
        if (R3_AT == 2 && tid == 0) st64(ws, tag, WS_R3 + bi * 128);
        STAMP(6);
        v4u vf[16];
        float mx;
        {
            const f32x4 m0 = *(LAS f32x4*)(L + a_mx), m1 = *(LAS f32x4*)(L + a_mx + 16);
            fence_sched();
#pragma unroll
            for (int i = 0; i < TR_EARLY; ++i) vf[i] = lds_tr2(L, a_v[i >> 2][(i >> 1) & 1] + (i & 1) * 32768, a_v[i >> 2][(i >> 1) & 1] + (i & 1) * 32768 + 16384);
            fence_sched();
            mx = fmx(fmx(fmx(m0[0], m0[1]), fmx(m0[2], m0[3])), fmx(fmx(m1[0], m1[1]), fmx(m1[2], m1[3])));
            const float p0 = __builtin_amdgcn_exp2f(sc[0] - mx), p1 = __builtin_amdgcn_exp2f(sc[1] - mx), p2 = __builtin_amdgcn_exp2f(sc[2] - mx), p3 = __builtin_amdgcn_exp2f(sc[3] - mx);
            *(LAS v2u*)(L + a_pw) = (v2u){pk(p0, p1), pk(p2, p3)};
            *(LAS float*)(L + a_mw + (LB_L - LB_MX)) = x32sum(x16sum((p0 + p1) + (p2 + p3)));
        }
        if (R3_AT == 3) {
            if (W_PF && wave * 64 < W_PF) WAIT_VM(1, "+v"(fa)); else WAIT_VM(0, "+v"(fa));
            if (A_CLEAN && wave == 0 && !flags_ge(fa, tag, 3)) wait_flags<0, 3>(ws, o_p1, tag);
        }
        barrier_lds();
        if (R3_AT == 3 && tid == 0) st64(ws, tag, WS_R3 + bi * 128);
        STAMP(7);
        {
            v4u pf[4];
#pragma unroll
            for (int s = 0; s < 4; ++s) pf[s] = lds128(L, a_pr + s * 1024);
            const f32x4 l0 = *(LAS f32x4*)(L + a_mx + (LB_L - LB_MX)), l1 = *(LAS f32x4*)(L + a_mx + (LB_L - LB_MX) + 16);
            fence_sched();
#pragma unroll
            for (int i = TR_EARLY; i < 16; ++i) vf[i] = lds_tr2(L, a_v[i >> 2][(i >> 1) & 1] + (i & 1) * 32768, a_v[i >> 2][(i >> 1) & 1] + (i & 1) * 32768 + 16384);
            fence_sched();
            if (wave == 7) st64(ws, (v2u){nos(__float_as_uint(mx)), nos(__float_as_uint(((l0[0] + l0[1]) + (l0[2] + l0[3])) + ((l1[0] + l1[1]) + (l1[2] + l1[3]))))}, o_ml);
            v4u o[2];
#pragma unroll
            for (int u = 0; u < 2; ++u) {
                f32x4 c0 = {0.f, 0.f, 0.f, 0.f}, c1 = {0.f, 0.f, 0.f, 0.f};
#pragma unroll
                for (int s = 0; s < 4; ++s) c0 = mfma32(vf[8 * u + s], pf[s], c0);
#pragma unroll
                for (int s = 0; s < 4; ++s) c1 = mfma32(vf[8 * u + 4 + s], pf[s], c1);
                auto s0 = __builtin_amdgcn_permlane16_swap(nos(pk(c0[0], c0[1])), nos(pk(c1[0], c1[1])), false, false);
                auto s1 = __builtin_amdgcn_permlane16_swap(nos(pk(c0[2], c0[3])), nos(pk(c1[2], c1[3])), false, false);
                o[u] = (v4u){s0[0], s1[0], s0[1], s1[1]};
            }
            v4u x0, x1;
#pragma unroll
            for (int k = 0; k < 4; ++k) {
                x0[k] = __builtin_amdgcn_update_dpp(o[0][k], o[1][k], ROR8, 0xf, 0xc, false);
                x1[k] = __builtin_amdgcn_update_dpp(o[1][k], o[0][k], ROR8, 0xf, 0x3, false);
            }
            st128(ws, x0, o_h3); st128(ws, x1, o_h3b);
        }
        STAMP(8);
        if (wave >= 6) {
#pragma unroll
            for (int k = 0; k < 4; ++k) asm volatile("buffer_load_dword %0, %1, %2, 0 offen offset:%3 sc1" : "+v"(m1[k]) : "v"(o1), "s"(wsd), "i"(k * 1152) : "memory");
        }
        barrier_lds();
        if (wave < 6) {
            STAMPD(1);
            if (W_DELAY) __builtin_amdgcn_s_sleep(W_DELAY);
            const int third = wave >> 1;
            const unsigned so = (wave & 1) * 9216 + third * 384, lo = lds0 + LB_W + (wave & 1) * 9216 + third * 3072;
            if (!(wave & 1)) {
#pragma unroll
                for (int i = 0; i < 3; ++i) dma16w(srd_2, v_w, so + 4 * 9216 + i * 128, lo + 8 * 9216 + i * 1024);
            }
#pragma unroll
            for (int i = 0; i < 6; ++i) dma16w(srd_2, v_w, so + (i / 3) * 18432 + (i % 3) * 128, lo + 4 * 9216 + (i / 3) * 18432 + (i % 3) * 1024);
#pragma unroll
            for (int i = 0; i < 6; ++i) dma16w(srd_o, v_w, so + (i / 3) * 18432 + (i % 3) * 128, lo + (i / 3) * 18432 + (i % 3) * 1024);
            STAMPD(2);
            wait_vm<6>();
            lds_arrive(L, LB_CNT, lane);
            lds_wait(L, LB_CNT, 8);
            f32x4 sel;
            {
                v4u wm[6]; v2u wt[6];
                const v4u f1 = lds128(L, a_h + 64);
                const v2u ft = *(LAS v2u*)(L + a_ht);
#pragma unroll
                for (int i = 0; i < 6; ++i) {
                    const unsigned aw = (i & 1) ? a_w1 : a_w, at = (i & 1) ? a_wt1 : a_wt, o = (i >> 1) * 1024;
                    wm[i] = lds_tr2(L, aw + 36864 + o, aw + 36864 + o + 512); wt[i] = lds_tr(L, at + o);
                }
                fence_sched();
#pragma unroll
                for (int i = 0; i < 6; ++i) {
                    const f32x4 c = mfma16(wt[i], ft, mfma32(wm[i], f1, (f32x4){0.f, 0.f, 0.f, 0.f}));
#pragma unroll
                    for (int k = 0; k < 4; ++k) sel[k] = (i == 0 || (n >> 1) == i) ? c[k] : sel[k];
                }
                pin(sel);
            }
            wait_vm<0>();
            STAMPD(3);
            lds_arrive(L, LB_CNT + 4, lane);
            lds_wait(L, LB_CNT + 4, 6);
            STAMP(9);
            v4u wf[6];
#pragma unroll
            for (int i = 0; i < 6; ++i) { const unsigned aw = (i & 1) ? a_w1 : a_w, o = (i >> 1) * 1024; wf[i] = lds_tr2(L, aw + o, aw + o + 512); }
            barrier_lds();
            STAMP(10);
            v4u f0[6];
#pragma unroll
            for (int i = 0; i < 6; ++i) f0[i] = lds128(L, a_c[i]);
            fence_sched();
#pragma unroll
            for (int i = 0; i < 6; ++i) sel = mfma32(wf[i], f0[i], sel);
            st128(ws, (v4u){nos(__float_as_uint(sel[0])), nos(__float_as_uint(sel[1])), nos(__float_as_uint(sel[2])), nos(__float_as_uint(sel[3]))}, o_h5);
        } else {
            const v2u tg = *(LAS v2u*)(L + LB_TAG);
            const float gate = gate_of(xv, gv);
            WAIT_VM(0, "+v"(m1));
#pragma unroll 1
            for (unsigned spins = 0; spins < SPIN_CAP && __any(umax4(m1) == SENT); ++spins) {
                asm volatile("" ::: "memory");
                m1 = (v4u){ld32(ws, o1), ld32(ws, o1 + 1152), ld32(ws, o1 + 2304), ld32(ws, o1 + 3456)};
            }
            const float v = bfr(bfr((__uint_as_float(m1.x) + __uint_as_float(m1.y)) + (__uint_as_float(m1.z) + __uint_as_float(m1.w))) + bflo(b1v));
            const float u = 0.7978845608028654f * (v + 0.044715f * v * v * v);
            const float hv = bfr(1.f - gate) * (v / (1.f + __expf(-2.f * u)));
            if (lane < 48) *(LAS u16*)(L + LB_H + (wave & 1) * 256 + 64 + lane * 2) = lane < 36 ? (u16)pk(hv, hv) : (u16)0;
            asm volatile("s_waitcnt lgkmcnt(0)" ::: "memory");
            lds_arrive(L, LB_CNT, lane);
            STAMP(14);
            if (H4_DELAY) __builtin_amdgcn_s_sleep(H4_DELAY);
            v4u t;
#pragma unroll 1
            for (unsigned spins = 0;; ++spins) {
                asm volatile("" ::: "memory");
                t = ld128(ws, o4);
                if (!__any(tag_miss(t, tg) != 0) || spins >= SPIN_CAP) break;
            }
            float c[4];
#pragma unroll
            for (int k = 0; k < 4; ++k) { c[k] = bfhi(t[k]); c[k] += dpp<ROR8>(c[k]); c[k] = gate * x16sum(c[k]); }
            *(LAS v2u*)(L + a_cx) = (v2u){pk(c[0], c[1]), pk(c[2], c[3])};
            STAMP(15);
            barrier_lds();
        }
        STAMP(11);
    }
    if (lane < 16) __hip_atomic_store((GAS u64*)(wsp + WS_DBG) + (bid * 8 + wave) * 16 + lane, (u64)stampv, __ATOMIC_RELAXED, __HIP_MEMORY_SCOPE_AGENT);
}

template <int MODE, int ROLE, int IDX>
__global__ void spin_kernel(char* wsp) {
    u64 tin64; asm volatile("s_memrealtime %0\n\ts_waitcnt lgkmcnt(0)" : "=s"(tin64) :: "memory");
    const unsigned tin = (unsigned)tin64;
    __shared__ unsigned sm[2][256];
    const GAS u64* d = (const GAS u64*)(wsp + WS_DBG);
    unsigned a0 = 0, v = 0xFFFFFFFFu;
    const int bid = threadIdx.x;
    for (int w = 0; w < 8; ++w) {
        const GAS u64* s = d + (bid * 8 + w) * 16;
        unsigned age = tin - (unsigned)s[0]; a0 = age > a0 ? age : a0;
        for (int i = 0; i < 16; ++i) if (MODE != 3 || (i == IDX && ((bid >> 3) >= 16) == ROLE)) { unsigned x = (unsigned)s[i]; if (x) { age = tin - x; v = age < v ? age : v; } }
    }
    sm[0][bid] = a0; sm[1][bid] = v;
    __syncthreads();
    if (bid == 0) {
        for (int i = 0; i < 256; ++i) { a0 = sm[0][i] > a0 ? sm[0][i] : a0; v = sm[1][i] < v ? sm[1][i] : v; }
        u64 span = MODE == 0 ? v : MODE == 2 ? 1 : a0 - v;
        if (span > 5000) span = 5000;
        u64 t, t1; asm volatile("s_memrealtime %0\n\ts_waitcnt lgkmcnt(0)" : "=s"(t) :: "memory");
        do { asm volatile("s_memrealtime %0\n\ts_waitcnt lgkmcnt(0)" : "=s"(t1) :: "memory"); } while (t1 < t + span * 100);
    }
}
extern "C" void kernel_launch(void* const* d_in, const int* in_sizes, int n_in, void* d_out, int out_size, void* d_ws, size_t ws_size, hipStream_t stream) {
    fused_decode_kernel<<<256, NT, 0, stream>>>((const u16*)d_in[0], (const u16*)d_in[1], (const u16*)d_in[2], (const u16*)d_in[3], (const u16*)d_in[4],
                                                (const u16*)d_in[5], (const u16*)d_in[7], (const u16*)d_in[8], (const u16*)d_in[9], (const u16*)d_in[10],
                                                (const u16*)d_in[11], (u16*)d_out, (char*)d_ws);
    spin_kernel<2, 0, 0><<<1, 256, 0, stream>>>((char*)d_ws);
}
```

```cpp
#define DBG_STAMP
#define STAMP_MASK 0x8001
#define PF_T 115
#define H1_T 225
#define QA_T 360
#define A_T 330
#include <hip/hip_runtime.h>
#include <hip/hip_bf16.h>
#include <stdint.h>

typedef unsigned short u16;
typedef unsigned long long u64;
typedef unsigned v4u __attribute__((ext_vector_type(4)));
typedef unsigned v2u __attribute__((ext_vector_type(2)));
typedef int v4i __attribute__((ext_vector_type(4)));
typedef float f32x4 __attribute__((ext_vector_type(4)));
typedef float f32x2 __attribute__((ext_vector_type(2)));
typedef short s16x4 __attribute__((ext_vector_type(4)));
typedef __bf16 bf16x8 __attribute__((ext_vector_type(8)));
typedef __bf16 bf16x2 __attribute__((ext_vector_type(2)));
#define GAS __attribute__((address_space(1)))
#define LAS __attribute__((address_space(3)))
#define DI __device__ __forceinline__
typedef __amdgpu_buffer_rsrc_t rsrc_t;

constexpr int NT = 512;
constexpr unsigned SENT = 0xFFFFFFFFu;
constexpr int SC1 = 16;
constexpr unsigned SPIN_CAP = 1u << 17;
constexpr u64 TAG_STEP = 0x9E3779B97F4A7C14ull;

constexpr int WS_E = 0;
constexpr int WS_P1 = 4096;
constexpr int WS_R3 = WS_P1 + 16384;
constexpr int WS_P2 = WS_R3 + 16384;
constexpr int WS_H1 = WS_P2 + 16384;
constexpr int WS_QA = WS_H1 + 128 * 1024;
constexpr int WS_M1 = WS_QA + 65536;
constexpr int WS_ML = WS_M1 + 128 * 1152;
constexpr int WS_H3 = WS_ML + 16384;
constexpr int WS_H5 = WS_H3 + 64 * 32768;
constexpr int WS_END = WS_H5 + 256 * 4608;
constexpr unsigned NOWHERE = 0x7FFFFF00u;
constexpr int WS_DBG = 8 << 20;

constexpr int LA_WQ = 0, LA_WUK = 36864, LA_WO = 0, LA_W1 = 73728, LA_WUV = 115200, LA_QF = 147968, LA_CMB = LA_QF, LA_COL = 156160, LA_TAG = 156288, LA_G = 156304, LA_ZERO = 156320  ,
              LA_X = 156416, LA_RED = 157056, LA_CTX = 158080;
constexpr int LB_CKV = 0, LB_W = 0, LB_Q = 131072, LB_P = 147456, LB_MX = 151552, LB_L = 152064, LB_H = 152576, LB_TAG = 153088, LB_CNT = 153104, LB_ZERO = 153216;
constexpr int LDS_BYTES = 158464;
constexpr int CODE_LINES = 160;

#define STAMP(i) do { if (!((STAMP_MASK >> (i)) & 1)) break; __builtin_amdgcn_sched_barrier(0); { u64 t_; asm volatile("s_memrealtime %0\n\ts_waitcnt lgkmcnt(0)" : "=s"(t_) :: "memory"); asm volatile("v_writelane_b32 %0, %1, %2" : "+v"(stampv) : "s"((unsigned)t_), "i"(i)); } __builtin_amdgcn_sched_barrier(0); } while (0)
#define STAMPE(i) STAMP(i)
#define STAMPD(i) do {} while (0)

DI v4i mk_srd(const void* p, unsigned bytes) {
    const u64 a = (u64)p;
    return (v4i){(int)(unsigned)a, (int)((unsigned)(a >> 32) & 0xFFFFu), (int)bytes, 0x00020000};
}
DI void dma16(v4i srd, unsigned voff, unsigned soff, unsigned lds) {
    asm volatile("s_mov_b32 m0, %0\n\ts_nop 0\n\tbuffer_load_dwordx4 %1, %2, %3 offen lds" :: "s"(lds), "v"(voff), "s"(srd), "s"(soff) : "memory");
}
#define W_NT 0
DI void dma16w(v4i srd, unsigned voff, unsigned soff, unsigned lds) {
    if (W_NT) asm volatile("s_mov_b32 m0, %0\n\ts_nop 0\n\tbuffer_load_dwordx4 %1, %2, %3 offen nt lds" :: "s"(lds), "v"(voff), "s"(srd), "s"(soff) : "memory");
    else dma16(srd, voff, soff, lds);
}
DI void touch(v4i srd, unsigned voff) { asm volatile("buffer_load_dword v255, %0, %1, 0 offen" :: "v"(voff), "s"(srd) : "memory", "v255"); }
DI u64 now() { u64 t; asm volatile("s_memrealtime %0\n\ts_waitcnt lgkmcnt(0)" : "=s"(t) :: "memory"); return t; }
DI void wait_until(u64 t) { while (now() < t) __builtin_amdgcn_s_sleep(2); }
DI v4u ald128(v4i srd, unsigned voff) { v4u r; asm volatile("buffer_load_dwordx4 %0, %1, %2, 0 offen sc1" : "=v"(r) : "v"(voff), "s"(srd) : "memory"); return r; }
DI v4u ald128p(v4i srd, unsigned voff) { v4u r; asm volatile("buffer_load_dwordx4 %0, %1, %2, 0 offen" : "=v"(r) : "v"(voff), "s"(srd) : "memory"); return r; }
DI v2u ald64p(v4i srd, unsigned voff) { v2u r; asm volatile("buffer_load_dwordx2 %0, %1, %2, 0 offen" : "=v"(r) : "v"(voff), "s"(srd) : "memory"); return r; }
DI void ald_e(v2u& r, v4i srd) { asm volatile("buffer_load_dwordx2 %0, off, %1, 0 sc1" : "+v"(r) : "s"(srd) : "memory"); }
DI void ald64_into(v2u& r, v4i srd, unsigned voff) { asm volatile("buffer_load_dwordx2 %0, %1, %2, 0 offen sc1" : "+v"(r) : "v"(voff), "s"(srd) : "memory"); }
#define WAIT_VM(N, ...) asm volatile("s_waitcnt vmcnt(" #N ")" : __VA_ARGS__ :: "memory")
DI v2u ald64(v4i srd, unsigned voff) { v2u r; asm volatile("buffer_load_dwordx2 %0, %1, %2, 0 offen sc1" : "=v"(r) : "v"(voff), "s"(srd) : "memory"); return r; }
DI bool flags_ok(v2u f, v2u tag) { return __all((f.x == tag.x) & (f.y == tag.y)); }
DI bool flags_ge(v2u f, v2u tag, unsigned stage) { return __all(((f.x | 3u) == (tag.x | 3u)) & ((f.x & stage) == stage) & (f.y == tag.y)); }
DI v2u mk_tag(v2u e) { const u64 t = (((u64)e.y << 32) | e.x) + TAG_STEP; return (v2u){(unsigned)t & ~3u, (unsigned)(t >> 32)}; }
template <class T> DI void pin(T& v) { asm volatile("" : "+v"(v)); }
DI void fence_sched() { __builtin_amdgcn_sched_barrier(0); }
template <int N> DI void wait_vm() { asm volatile("s_waitcnt vmcnt(%0)" :: "i"(N) : "memory"); }
DI void barrier_lds() { asm volatile("s_waitcnt lgkmcnt(0)\n\ts_barrier" ::: "memory"); }
DI f32x4 mfma32(v4u a, v4u b, f32x4 c) { return __builtin_amdgcn_mfma_f32_16x16x32_bf16(__builtin_bit_cast(bf16x8, a), __builtin_bit_cast(bf16x8, b), c, 0, 0, 0); }
DI f32x4 mfma16(v2u a, v2u b, f32x4 c) { return mfma32((v4u){a.x, a.y, 0u, 0u}, (v4u){b.x, b.y, 0u, 0u}, c); }
DI v2u lds_tr(LAS char* L, unsigned off) { return __builtin_bit_cast(v2u, __builtin_amdgcn_ds_read_tr16_b64_v4i16((LAS s16x4*)(L + off))); }
DI v4u lds_tr2(LAS char* L, unsigned o0, unsigned o1) { const v2u a = lds_tr(L, o0), b = lds_tr(L, o1); return (v4u){a.x, a.y, b.x, b.y}; }
DI v4u lds128(LAS char* L, unsigned off) { return *(LAS v4u*)(L + off); }
DI unsigned pk(float lo, float hi) { return __builtin_bit_cast(unsigned, __builtin_convertvector((f32x2){lo, hi}, bf16x2)); }
#define USE_NOS 0
DI unsigned nos(unsigned v) { return !USE_NOS || v < 0xFFFFFFFEu ? v : 0xFFFFFFFEu; }
DI float bflo(unsigned v) { return __uint_as_float(v << 16); }
DI float bfhi(unsigned v) { return __uint_as_float(v & 0xFFFF0000u); }
DI float bfr(float f) { return bflo(pk(f, f)); }
template <int CTRL> DI float dpp(float x) { return __builtin_bit_cast(float, __builtin_amdgcn_mov_dpp(__builtin_bit_cast(int, x), CTRL, 0xf, 0xf, true)); }
DI float fmx(float a, float b) { return __builtin_elementwise_maximum(a, b); }
constexpr int XOR1 = 0xB1, XOR2 = 0x4E, ROR4 = 0x124, ROR8 = 0x128;
DI float x16max(float x) { auto s = __builtin_amdgcn_permlane16_swap(__float_as_uint(x), __float_as_uint(x), false, false); return fmx(__uint_as_float(s[0]), __uint_as_float(s[1])); }
DI float x32max(float x) { auto s = __builtin_amdgcn_permlane32_swap(__float_as_uint(x), __float_as_uint(x), false, false); return fmx(__uint_as_float(s[0]), __uint_as_float(s[1])); }
DI float x16sum(float x) { auto s = __builtin_amdgcn_permlane16_swap(__float_as_uint(x), __float_as_uint(x), false, false); return __uint_as_float(s[0]) + __uint_as_float(s[1]); }
DI float x32sum(float x) { auto s = __builtin_amdgcn_permlane32_swap(__float_as_uint(x), __float_as_uint(x), false, false); return __uint_as_float(s[0]) + __uint_as_float(s[1]); }
DI unsigned umax2(unsigned a, unsigned b) { return a > b ? a : b; }
DI unsigned umax4(v4u v) { return umax2(umax2(v.x, v.y), umax2(v.z, v.w)); }
DI void st128(rsrc_t rs, v4u v, unsigned off) { __builtin_amdgcn_raw_buffer_store_b128(v, rs, off, 0, SC1); }
DI void st64(rsrc_t rs, v2u v, unsigned off) { __builtin_amdgcn_raw_buffer_store_b64(v, rs, off, 0, SC1); }
DI v4u ld128(rsrc_t rs, unsigned off) { return __builtin_amdgcn_raw_buffer_load_b128(rs, off, 0, SC1); }
DI v2u ld64(rsrc_t rs, unsigned off) { return __builtin_amdgcn_raw_buffer_load_b64(rs, off, 0, SC1); }
DI unsigned ld32(rsrc_t rs, unsigned off) { return __builtin_amdgcn_raw_buffer_load_b32(rs, off, 0, SC1); }

template <int SLP, unsigned STAGE = 0> DI void wait_flags(rsrc_t rs, unsigned off, v2u tag) {
#pragma unroll 1
    for (unsigned spins = 0; spins < SPIN_CAP; ++spins) {
        asm volatile("" ::: "memory");
        const v2u f = ld64(rs, off);
        if (flags_ge(f, tag, STAGE)) break;
        if (SLP) __builtin_amdgcn_s_sleep(SLP);
    }
    asm volatile("" ::: "memory");
}
template <int GAP> DI void wait_flags3(v4i srd, unsigned off, v2u want) {
    unsigned cnt;
#define LOOK(R) "buffer_load_dwordx2 " R ", %1, %2, 0 offen sc1\n\t"
#define SEEN(R) "s_waitcnt vmcnt(2)\n\tv_cmp_eq_u64_e32 vcc, " R ", %3\n\ts_cmp_eq_u64 vcc, exec\n\ts_cbranch_scc1 2f\n\t"
    asm volatile(LOOK("v[248:249]") "s_sleep %5\n\t" LOOK("v[250:251]") "s_sleep %5\n\ts_mov_b32 %0, %4\n"
                 "1:\n\t" LOOK("v[252:253]") SEEN("v[248:249]") LOOK("v[248:249]") SEEN("v[250:251]") LOOK("v[250:251]") SEEN("v[252:253]")
                 "s_sub_u32 %0, %0, 1\n\ts_cbranch_scc0 1b\n"
                 "2:"
                 : "=&s"(cnt) : "v"(off), "s"(srd), "v"(want), "i"(SPIN_CAP), "i"(GAP)
                 : "memory", "vcc", "scc", "v248", "v249", "v250", "v251", "v252", "v253");
#undef LOOK
#undef SEEN
}
template <int N> DI bool landed(const v4u (&r)[N]) {
    unsigned m = 0;
#pragma unroll
    for (int i = 0; i < N; ++i) m = umax2(m, umax4(r[i]));
    return !__any(m == SENT);
}
template <int N, int SLP = 0> DI void regather(rsrc_t rs, const unsigned (&off)[N], v4u (&r)[N]) {
#pragma unroll 1
    for (unsigned spins = 0; spins < SPIN_CAP; ++spins) {
        if (SLP) __builtin_amdgcn_s_sleep(SLP);
        asm volatile("" ::: "memory");
#pragma unroll
        for (int i = 0; i < N; ++i) {
            const bool miss = umax4(r[i]) == SENT;
            const v4u v = ld128(rs, miss ? off[i] : NOWHERE);
            r[i] = miss ? v : r[i];
        }
        if (landed<N>(r)) break;
    }
}

DI void lds_arrive(LAS char* L, int off, int lane) { if (lane == 0) __hip_atomic_fetch_add((LAS unsigned*)(L + off), 1u, __ATOMIC_RELAXED, __HIP_MEMORY_SCOPE_WORKGROUP); }
DI void lds_wait(LAS char* L, int off, unsigned count) {
#pragma unroll 1
    for (unsigned spins = 0; spins < (SPIN_CAP << 4); ++spins) {
        asm volatile("" ::: "memory");
        if (__builtin_amdgcn_readfirstlane(*(volatile LAS unsigned*)(L + off)) >= count) break;
    }
    asm volatile("" ::: "memory");
}
DI v4u tagged4(f32x4 c, v2u tag) {
    const unsigned p01 = pk(c[0], c[1]), p23 = pk(c[2], c[3]);
    return (v4u){__builtin_amdgcn_perm(p01, tag.x, 0x05040100), __builtin_amdgcn_perm(p01, tag.x, 0x07060302),
                 __builtin_amdgcn_perm(p23, tag.y, 0x05040100), __builtin_amdgcn_perm(p23, tag.y, 0x07060302)};
}
DI unsigned tag_miss(v4u t, v2u tag) { return (__builtin_amdgcn_perm(t.y, t.x, 0x05040100) ^ tag.x) | (__builtin_amdgcn_perm(t.w, t.z, 0x05040100) ^ tag.y); }
DI float gate_of(const v4u (&xv)[2], const v4u (&gv)[2]) {
    float s = 0.f;
#pragma unroll
    for (int i = 0; i < 2; ++i)
#pragma unroll
        for (int k = 0; k < 4; ++k) { const unsigned pr = pk(bflo(xv[i][k]) * bflo(gv[i][k]), bfhi(xv[i][k]) * bfhi(gv[i][k])); s += bflo(pr) + bfhi(pr); }
    s += dpp<XOR1>(s); s += dpp<XOR2>(s); s += dpp<ROR4>(s); s += dpp<ROR8>(s); s = x32sum(x16sum(s));
    return bfr(1.f / (1.f + __expf(-bfr(s))));
}

#define A_DELAY 28
#define PF_DELAY 12
#define H1_DELAY 8
#define H3_DELAY 0
#define H5_DELAY 8
#define FA_AT 9
#define FA_USE 14
#define FAST_Q 1
#define Q_SLP 2
#define TR_EARLY 6
#define W_PF 0
#define R3_AT 2
#define W_DELAY 18
#define DRY 1
#define CL_FIRST 0
#define H4_DELAY 0
#define C1_GATE 0
#define APF 224
#define UK_PF 1
#define Q_DELAY 0
#define A_CLEAN 0
#define CK_MIX 8
#define CK_BAR 0
#define OUT_AUX 0
#define APF1 0
#define POLL3 6
#define MK_QK 0
#define TR_QK 0
#define PV_PRIO 1
#define WO_A 2
#define WO_B 4
#define BPF 1
#define W1E 0
#define W1_T 275
#define CODE_PF 1
__global__ void __launch_bounds__(NT) __attribute__((amdgpu_waves_per_eu(2, 2)))
fused_decode_kernel(const u16* __restrict__ xp, const u16* __restrict__ ckv, const u16* __restrict__ wq, const u16* __restrict__ wuk,
                    const u16* __restrict__ wuv, const u16* __restrict__ wo, const u16* __restrict__ w1, const u16* __restrict__ b1,
                    const u16* __restrict__ w2, const u16* __restrict__ b2, const u16* __restrict__ gw, u16* __restrict__ out, char* wsp) {
    __shared__ __attribute__((aligned(1024))) char smem[LDS_BYTES];
    LAS char* L = (LAS char*)smem;
    const unsigned lds0 = (unsigned)(size_t)L;
    unsigned stampv = 0;
    STAMP(0);
    const u64 t0 = now();
    const int bid = blockIdx.x, tid = threadIdx.x, lane = tid & 63;
    const int xcd = bid & 7, slot = bid >> 3;
    if (CODE_PF) {
        u64 pc0; asm volatile("s_getpc_b64 %0" : "=s"(pc0));
        if (slot + 32 * tid < CODE_LINES)
            asm volatile("global_load_dword v255, %0, %1" :: "v"((slot + 32 * tid) * 128), "s"(pc0 & ~127ull) : "memory", "v255");
    }
    asm volatile("" :: "s"(xp), "s"(ckv), "s"(wq), "s"(wuk), "s"(wuv), "s"(wo), "s"(w1), "s"(b1), "s"(w2), "s"(b2), "s"(gw), "s"(out), "s"(wsp));
    const int wave = __builtin_amdgcn_readfirstlane(tid >> 6);
    const int g = lane >> 4, n = lane & 15, q = (lane >> 2) & 3, p = lane & 3;
    const rsrc_t ws = __builtin_amdgcn_make_buffer_rsrc(wsp, 0, WS_END, 0x00020000);
    const v4i wsd = mk_srd(wsp, WS_END);
    const v4u CLEAN = {SENT, SENT, SENT, SENT};

    if (__builtin_expect(slot < 16, 1)) {
        const int a = xcd * 16 + slot, h = a >> 2, j = a & 3;
        asm volatile("s_nop 4" ::: "memory");
        const unsigned v_t = g * 8192 + 16 * (n ^ ((4 * ((wave >> 1) & 1) + g) << 1));
        {
            const v4i srd = mk_srd(wq + (size_t)j * 144 * 4096 + h * 128, 143 * 8192 + 256);
#pragma unroll
            for (int i = 0; i < 5; ++i)
                if (i < 4 || wave < 4) dma16w(srd, v_t, (wave + 8 * i) * 32768, lds0 + LA_WQ + (wave + 8 * i) * 1024);
        }
        v2u e = {0u, 0u};
        if (wave == 7) {
            ald_e(e, wsd);
            if (lane < 36) dma16(mk_srd(xp, 2304), (lane >= 18 ? 1152 - 288 : 0) + j * 288 + lane * 16, 0, lds0 + LA_X);
            if (lane < 32) st128(ws, CLEAN, WS_QA + g * 32768 + h * 1024 + j * 256 + n * 16);
        }
        v4u xv[2] = {{0u, 0u, 0u, 0u}, {0u, 0u, 0u, 0u}}, gv[2] = {{0u, 0u, 0u, 0u}, {0u, 0u, 0u, 0u}};
        unsigned b2v = 0;
        if (wave == 6 && bid < 36) {
            const v4i sx = mk_srd(xp + (bid / 18) * 576, 1152), sg = mk_srd(gw, 1152);
#pragma unroll
            for (int k = 0; k < 2; ++k) {
                asm volatile("buffer_load_dwordx4 %0, %1, %2, 0 offen offset:%3" : "+v"(xv[k]) : "v"(lane * 16), "s"(sx), "i"(k * 1024) : "memory");
                asm volatile("buffer_load_dwordx4 %0, %1, %2, 0 offen offset:%3" : "+v"(gv[k]) : "v"(lane * 16), "s"(sg), "i"(k * 1024) : "memory");
            }
            asm volatile("buffer_load_ushort %0, %1, %2, 0 offen" : "+v"(b2v) : "v"((lane & 31) * 2), "s"(mk_srd(b2 + bid * 32 - (bid / 18) * 576, 64)) : "memory");
        }
        const v4i srd_uk = mk_srd(wuk + (size_t)j * 128 * 4096 + h * 128, 127 * 8192 + 256);
        unsigned v_uk[4], a_uk[4];
#pragma unroll
        for (int i = 0; i < 4; ++i) {
            v_uk[i] = g * 8192 + 16 * (n ^ (4 * i + g)); a_uk[i] = LA_WUK + (16 * wave + n) * 256 + 16 * ((4 * i + g) ^ n);
            pin(v_uk[i]); pin(a_uk[i]);
        }
#define UK_ROWS() do { _Pragma("unroll") for (int i = 0; i < 4; ++i) dma16w(srd_uk, v_uk[i], (16 * wave + 4 * i) * 8192, lds0 + LA_WUK + (16 * wave + 4 * i) * 256); } while (0)
        unsigned a_x = LA_X + (n & 1) * 288 + 16 * g, a_xt = LA_X + (n & 1) * 288 + 256 + 8 * g;
        unsigned a_q = (8 * g + q) * 256 + 16 * ((2 * wave + (p >> 1)) ^ ((q + 4 * (g & 1)) << 1)) + 8 * (p & 1);
        unsigned a_qt = LA_WQ + (128 + 4 * g + q) * 256 + 16 * ((2 * wave + (p >> 1)) ^ ((q + 4 * (g >> 1)) << 1)) + 8 * (p & 1);
        unsigned o_h1 = n < 2 ? WS_H1 + a * 1024 + wave * 128 + n * 64 + g * 16 : NOWHERE;
        unsigned o_g1 = WS_H1 + (4 * h + ((n >> 1) & 3)) * 1024 + (wave & 1) * 512 + (g >> 1) * 128 + (n & 1) * 64 + (g & 1) * 32;
        unsigned o_qa = n < 2 ? WS_QA + n * 32768 + h * 1024 + (128 * j + 16 * wave + 4 * g) * 2 : NOWHERE;
        const v4i srd_w1 = mk_srd(w1 + (size_t)j * 144 * 4608 + h * 144, 143 * 9216 + 288);
        unsigned v_w1 = (lane / 18) * 9216 + (lane % 18) * 16;
        pin(v_w1);
        unsigned a_l16 = LA_QF + lane * 16;
        unsigned o_cl = tid < 200 ? WS_M1 + a * 1152 + (tid - 128) * 16 : tid < 488 ? WS_H5 + bid * 4608 + (tid - 200) * 16 : NOWHERE;
        pin(o_cl);
        pin(a_x); pin(a_xt); pin(a_q); pin(a_qt); pin(o_h1); pin(o_g1); pin(o_qa); pin(a_l16);
        WAIT_VM(0, "+v"(e), "+v"(xv[0]), "+v"(xv[1]), "+v"(gv[0]), "+v"(gv[1]), "+v"(b2v));
        if (tid == 448) *(LAS v2u*)(L + LA_TAG) = mk_tag(e);
        STAMP(1);
        barrier_lds();
        const v2u tag = *(LAS v2u*)(L + LA_TAG);
        v4u xf[4];
#pragma unroll
        for (int s = 0; s < 4; ++s) xf[s] = lds128(L, a_x + 64 * s);
        const v2u xt = *(LAS v2u*)(L + a_xt);
        {
            v4u wf[4];
#pragma unroll
            for (int s = 0; s < 4; ++s) wf[s] = lds_tr2(L, LA_WQ + a_q + s * 8192, LA_WQ + a_q + s * 8192 + 1024);
            const v2u wt = lds_tr(L, a_qt);
            fence_sched();
            f32x4 c = {0.f, 0.f, 0.f, 0.f};
#pragma unroll
            for (int s = 0; s < 4; ++s) c = mfma32(wf[s], xf[s], c);
            c = mfma16(wt, xt, c);
            st128(ws, tagged4(c, tag), o_h1);
        }
        if (wave >= 2) {
            if (tid == 448) st128(ws, (v4u){tag.x, tag.y, (unsigned)t0 + QA_T, 0u}, WS_P1 + a * 128);
            st128(ws, CLEAN, o_cl);
        }
        STAMP(2);
        if (wave < 2) {
            if (H1_DELAY) __builtin_amdgcn_s_sleep(H1_DELAY);
            wait_until(t0 + H1_T);
            v4u t[4];
#pragma unroll
            for (int s = 0; s < 4; ++s) asm volatile("buffer_load_dwordx4 %0, %1, %2, 0 offen offset:%3 sc1" : "=v"(t[s]) : "v"(o_g1), "s"(wsd), "i"((s >> 1) * 256 + (s & 1) * 16) : "memory");
            if (UK_PF) {
                asm volatile("s_barrier" ::: "memory");
                UK_ROWS();
                WAIT_VM(4, "+v"(t[0]), "+v"(t[1]), "+v"(t[2]), "+v"(t[3]));
            } else WAIT_VM(0, "+v"(t[0]), "+v"(t[1]), "+v"(t[2]), "+v"(t[3]));

            if (__builtin_expect(__any((tag_miss(t[0], tag) | tag_miss(t[1], tag) | tag_miss(t[2], tag) | tag_miss(t[3], tag)) != 0), 0)) {
#pragma unroll 1
                for (unsigned spins = 0; spins < SPIN_CAP; ++spins) {
                    asm volatile("" ::: "memory");
                    unsigned bad = 0;
#pragma unroll
                    for (int s = 0; s < 4; ++s) {
                        const bool miss = tag_miss(t[s], tag) != 0;
                        const v4u v = ld128(ws, miss ? o_g1 + (s >> 1) * 256 + (s & 1) * 16 : NOWHERE);
                        t[s] = miss ? v : t[s];
                    }
#pragma unroll
                    for (int s = 0; s < 4; ++s) bad |= tag_miss(t[s], tag);
                    if (!__any(bad != 0)) break;
                }
            }
#pragma unroll
            for (int s = 0; s < 2; ++s)
                *(LAS v4u*)(L + a_l16 + (2 * wave + s) * 1024) =
                    (v4u){__builtin_amdgcn_perm(t[2 * s].y, t[2 * s].x, 0x07060302), __builtin_amdgcn_perm(t[2 * s].w, t[2 * s].z, 0x07060302),
                          __builtin_amdgcn_perm(t[2 * s + 1].y, t[2 * s + 1].x, 0x07060302), __builtin_amdgcn_perm(t[2 * s + 1].w, t[2 * s + 1].z, 0x07060302)};
        } else {
            asm volatile("s_barrier" ::: "memory");
            UK_ROWS();
            if (W1E) {
                wait_until(t0 + W1_T);
                if (lane < 54) {
#pragma unroll
                    for (int i = 0; i < W1E; ++i) dma16(srd_w1, v_w1, (wave + 8 * i) * 27648, lds0 + LA_W1 + (wave + 8 * i) * 864);
                }
            }
        }
        if (W1E && wave >= 2) wait_vm<W1E>(); else wait_vm<0>();
        v4u uf[4];
#pragma unroll
        for (int s = 0; s < 4; ++s) uf[s] = lds128(L, a_uk[s]);
        barrier_lds();
        STAMP(4);
        {
            v4u qf[4];
#pragma unroll
            for (int s = 0; s < 4; ++s) qf[s] = lds128(L, a_l16 + s * 1024);
            fence_sched();
            if (tid == 448) st64(ws, tag, WS_P2 + a * 128);
            f32x4 c = {0.f, 0.f, 0.f, 0.f};
#pragma unroll
            for (int s = 0; s < 4; ++s) c = mfma32(uf[s], qf[s], c);
            constexpr float SC = 0.08838834764831845f * 1.4426950408889634f;
#pragma unroll
            for (int r = 0; r < 4; ++r) { c[r] += dpp<XOR2>(c[r]); c[r] += dpp<ROR4>(c[r]); c[r] *= SC; }
            st64(ws, (v2u){nos(pk(c[0], c[1])), nos(pk(c[2], c[3]))}, o_qa);
        }
        STAMP(5);
        float obias = 0.f;
        if (wave == 6 && bid < 36) { const float gt = gate_of(xv, gv); obias = bfr(1.f - gt) * bflo(b2v); pin(obias); *(LAS f32x2*)(L + LA_G) = (f32x2){gt, bfr(1.f - gt)}; }
        if (tid < 20) *(LAS unsigned*)(L + LA_ZERO + 4 * tid) = 0u;
        wait_until(t0 + A_T);
        if (lane < 54) {
            if (W1E && wave < 2) {
#pragma unroll
                for (int i = 0; i < W1E; ++i) dma16(srd_w1, v_w1, (wave + 8 * i) * 27648, lds0 + LA_W1 + (wave + 8 * i) * 864);
            }
#pragma unroll
            for (int i = W1E; i < 6; ++i) dma16(srd_w1, v_w1, (wave + 8 * i) * 27648, lds0 + LA_W1 + (wave + 8 * i) * 864);
        }
        asm volatile("s_barrier" ::: "memory");
        const int cs = j >> 1, ds = j & 1;
        const unsigned sub_c = 16 * ((lane & 7) ^ ((((lane >> 4) & 1) | ((wave & 1) << 1)) << 1));
        {
            const v4i srd = mk_srd(wuv + (size_t)cs * 256 * 4096 + h * 128 + ds * 64, 255 * 8192 + 128);
#pragma unroll
            for (int i = 0; i < 4; ++i) dma16w(srd, (lane >> 3) * 8192 + sub_c, (wave + 8 * i) * 65536, lds0 + LA_WUV + (wave + 8 * i) * 1024);
        }
        const v4i srd_wo = mk_srd(wo + (size_t)(h * 128 + ds * 64) * 576, 64 * 1152);
        unsigned v_wo = (lane >> 3) * 1152 + sub_c;
        pin(v_wo);
#define WO_PIECE(i) do { const int G = (i) + 5 * cs - ((i) + 5 * cs >= 9 ? 9 : 0); dma16(srd_wo, v_wo, wave * 9216 + G * 128, lds0 + LA_WO + wave * 9216 + G * 1024); } while (0)
        if (WO_A) asm volatile("s_barrier" ::: "memory");
#pragma unroll
        for (int i = 0; i < WO_A; ++i) WO_PIECE(i);
        STAMP(6);
        wait_vm<4 + WO_A>();
        barrier_lds();
        STAMP(7);
        {
            const unsigned a0 = LA_W1 + (8 * g + q) * 288 + 8 * p + 32 * wave, at = LA_W1 + (128 + 4 * g + q) * 288 + 8 * p + 32 * wave;
#pragma unroll
            for (int t = 0; t < 2; ++t) {
                if (t == 0 || wave == 2) {
                    f32x4 c = {0.f, 0.f, 0.f, 0.f};
#pragma unroll
                    for (int s = 0; s < 4; ++s) c = mfma32(lds_tr2(L, a0 + t * 192 + s * 9216, a0 + t * 192 + s * 9216 + 1152), xf[s], c);
                    c = mfma16(lds_tr(L, at + t * 192), xt, c);
                    const v4u v = {nos(__float_as_uint(c[0])), nos(__float_as_uint(c[1])), nos(__float_as_uint(c[2])), nos(__float_as_uint(c[3]))};
                    if (n < 2) st128(ws, v, WS_M1 + a * 1152 + n * 576 + (16 * wave + 96 * t + 4 * g) * 4);
                }
            }
        }
        if (WO_B > WO_A) asm volatile("s_barrier" ::: "memory");
#pragma unroll
        for (int i = WO_A; i < WO_B; ++i) WO_PIECE(i);
        STAMP(8);
        const int b = wave >> 2, hf = lane >> 5, li = lane & 31, k0 = 32 * b + 8 * (wave & 3) + 4 * hf, col = 4 * (wave & 3) + 2 * hf + b;
        unsigned o_h3 = WS_H3 + k0 * 32768 + h * 1024 + cs * 512 + li * 16, o_ml = WS_ML + (k0 + p) * 256 + h * 8;
        unsigned a_cw = LA_CMB + col * 512 + 16 * (li ^ col), a_col = LA_COL + 8 * col, a_colr = LA_COL + 8 * n;
        unsigned a_uv = LA_WUV + g * 1024 + q * 128 + 16 * ((2 * (wave & 3) + (p >> 1)) ^ (((q >> 1) | ((g & 1) << 1)) << 1)) + 8 * (p & 1);
        unsigned a_cr[4], a_c[5], o5 = WS_H5 + ((lane >> 3) + 8 * wave) * 4608 + bid * 128 + (lane & 7) * 16;
#pragma unroll
        for (int s = 0; s < 4; ++s) { a_cr[s] = LA_CMB + n * 512 + 16 * ((4 * s + g) ^ n); pin(a_cr[s]); }
        unsigned a_w = LA_WO + g * 9216 + (wave >> 2) * 1024 + q * 128 + 16 * ((2 * (wave & 3) + (p >> 1)) ^ (((q >> 1) | ((g & 1) << 1)) << 1)) + 8 * (p & 1), a_w2 = a_w + 36864;
#pragma unroll
        for (int i = 0; i < 5; ++i) { a_c[i] = (n >> 1) == i ? LA_CTX + (n & 1) * 128 + 16 * g : LA_ZERO; pin(a_c[i]); }
        unsigned a_c2 = (n >> 1) < 5 ? 64 : 0;
        unsigned a_cxw = LA_CTX + (n & 1) * 128 + (16 * wave + 4 * g) * 2;
        unsigned o_h5 = (n >> 1) < 5 - (wave >> 2) ? WS_H5 + bid * 4608 + (n & 1) * 2304 + ((wave >> 2) * 64 + (wave & 3) * 16 + 4 * g) * 4 + (n >> 1) * 512 : NOWHERE;
        unsigned a_rw = LA_RED + ((lane & 7) * 4 + 2 * hf + (g & 1)) * 32 + wave * 4, a_rr = LA_RED + li * 32;
        pin(o_h3); pin(o_ml); pin(a_cw); pin(a_col); pin(a_colr); pin(o5); pin(a_rw); pin(a_rr); pin(a_uv); pin(a_w); pin(a_w2); pin(a_cxw); pin(o_h5);
        (void)a_c2;
        if (wave == 0 || (wave == 1 && bid < 36)) wait_flags3<POLL3>(wsd, WS_R3 + (((h >> 4) ^ wave) * 64 + lane) * 128, tag);
        if (H3_DELAY) __builtin_amdgcn_s_sleep(H3_DELAY);
        wait_vm<1 + WO_B>();
        asm volatile("s_barrier" ::: "memory");
        STAMP(9);
        v4u r[4]; v2u ml;
#pragma unroll
        for (int i = 0; i < 4; ++i) asm volatile("buffer_load_dwordx4 %0, %1, %2, %3 offen sc1" : "=v"(r[i]) : "v"(o_h3), "s"(wsd), "s"(i * 32768) : "memory");
        asm volatile("buffer_load_dwordx2 %0, %1, %2, 0 offen sc1" : "=v"(ml) : "v"(o_ml), "s"(wsd) : "memory");
        if (WO_B < 9) asm volatile("s_barrier" ::: "memory");
#pragma unroll
        for (int i = WO_B; i < 9; ++i) WO_PIECE(i);
        v4u vf[8];
        if (wave < 4) {
#pragma unroll
            for (int s = 0; s < 8; ++s) vf[s] = lds_tr2(L, a_uv + s * 4096, a_uv + s * 4096 + 512);
        }
        asm volatile("s_waitcnt vmcnt(%5)" : "+v"(r[0]), "+v"(r[1]), "+v"(r[2]), "+v"(r[3]), "+v"(ml) : "i"(9 - WO_B) : "memory");
        STAMP(13);
#define H3_ALL() umax2(umax4((v4u){umax4(r[0]), umax4(r[1]), umax4(r[2]), umax4(r[3])}), umax2(ml.x, ml.y))
        if (__builtin_expect(__any(H3_ALL() == SENT), 0)) {
#pragma unroll 1
            for (unsigned spins = 0; spins < SPIN_CAP; ++spins) {
                asm volatile("" ::: "memory");
#pragma unroll
                for (int i = 0; i < 4; ++i) { const bool m = umax4(r[i]) == SENT; const v4u v = ld128(ws, m ? o_h3 + i * 32768 : NOWHERE); r[i] = m ? v : r[i]; }
                const bool m2 = umax2(ml.x, ml.y) == SENT;
                const v2u v2 = ld64(ws, m2 ? o_ml : NOWHERE);
                ml = m2 ? v2 : ml;
                if (!__any(H3_ALL() == SENT)) break;
            }
        }
        STAMP(10);
        {
            float mx = fmx(__uint_as_float(ml.x), dpp<XOR1>(__uint_as_float(ml.x))); mx = fmx(mx, dpp<XOR2>(mx));
            const float w = __builtin_amdgcn_exp2f(__uint_as_float(ml.x) - mx);
            const float wk[4] = {dpp<0x00>(w), dpp<0x55>(w), dpp<0xAA>(w), dpp<0xFF>(w)};
            float acc[8];
#pragma unroll
            for (int k = 0; k < 4; ++k) { acc[2 * k] = wk[0] * bflo(r[0][k]); acc[2 * k + 1] = wk[0] * bfhi(r[0][k]); }
#pragma unroll
            for (int i = 1; i < 4; ++i)
#pragma unroll
                for (int k = 0; k < 4; ++k) { acc[2 * k] = fmaf(wk[i], bflo(r[i][k]), acc[2 * k]); acc[2 * k + 1] = fmaf(wk[i], bfhi(r[i][k]), acc[2 * k + 1]); }
            *(LAS v4u*)(L + a_cw) = (v4u){pk(acc[0], acc[1]), pk(acc[2], acc[3]), pk(acc[4], acc[5]), pk(acc[6], acc[7])};
            float ls = w * __uint_as_float(ml.y); ls += dpp<XOR1>(ls); ls += dpp<XOR2>(ls);
            *(LAS f32x2*)(L + a_col) = (f32x2){mx, ls};
        }
        wait_vm<0>();
        barrier_lds();
        STAMP(11);
        v4u wf[5][2];
#define WO_FRAGS() do { _Pragma("unroll") for (int i = 0; i < 5; ++i) if (i < 4 || wave < 4) { \
            wf[i][0] = lds_tr2(L, a_w + i * 2048, a_w + i * 2048 + 512); wf[i][1] = lds_tr2(L, a_w2 + i * 2048, a_w2 + i * 2048 + 512); } } while (0)
        if (wave < 4) {
            const f32x2 cm = *(LAS f32x2*)(L + a_colr);
            v4u cf[8];
#pragma unroll
            for (int s = 0; s < 8; ++s) cf[s] = lds128(L, a_cr[s & 3] + 256 * (s >> 2));
            fence_sched();
            f32x4 c = {0.f, 0.f, 0.f, 0.f};
#pragma unroll
            for (int s = 0; s < 8; ++s) c = mfma32(vf[s], cf[s], c);
            float mx = fmx(cm[0], dpp<XOR2>(cm[0])); mx = fmx(mx, dpp<ROR4>(mx)); mx = fmx(mx, dpp<ROR8>(mx));
            const float wc = __builtin_amdgcn_exp2f(cm[0] - mx);
            float ls = wc * cm[1]; ls += dpp<XOR2>(ls); ls += dpp<ROR4>(ls); ls += dpp<ROR8>(ls);
            const float sc = wc * __builtin_amdgcn_rcpf(ls);
            fence_sched();
            WO_FRAGS();
            fence_sched();
#pragma unroll
            for (int k = 0; k < 4; ++k) { c[k] *= sc; c[k] += dpp<XOR2>(c[k]); c[k] += dpp<ROR4>(c[k]); c[k] += dpp<ROR8>(c[k]); }
            *(LAS v2u*)(L + a_cxw) = (v2u){pk(c[0], c[1]), pk(c[2], c[3])};
        } else WO_FRAGS();
        barrier_lds();
        STAMP(3);
        {
            v4u f0[5], f1[5];
#pragma unroll
            for (int i = 0; i < 5; ++i) if (i < 4 || wave < 4) { f0[i] = lds128(L, a_c[i]); f1[i] = lds128(L, a_c[i] + 64); }
            fence_sched();
            f32x4 o = {0.f, 0.f, 0.f, 0.f};
#pragma unroll
            for (int i = 0; i < 5; ++i) if (i < 4 || wave < 4) o = mfma32(wf[i][1], f1[i], mfma32(wf[i][0], f0[i], o));
            st128(ws, (v4u){nos(__float_as_uint(o[0])), nos(__float_as_uint(o[1])), nos(__float_as_uint(o[2])), nos(__float_as_uint(o[3]))}, o_h5);
        }
        STAMP(12);
        if (bid < 36) {
            const f32x2 gg = *(LAS f32x2*)(L + LA_G);
            if (H5_DELAY) __builtin_amdgcn_s_sleep(H5_DELAY);
            v4u r5[4];
            const unsigned off[4] = {o5 + 128 * 4608, o5 + 192 * 4608, o5, o5 + 64 * 4608};
#pragma unroll
            for (int i = 0; i < 4; ++i) r5[i] = ld128(ws, off[i]);
            if (!landed<4>(r5)) regather<4>(ws, off, r5);
            STAMP(14);
            float t[4];
#pragma unroll
            for (int k = 0; k < 4; ++k) t[k] = fmaf(gg[0], __uint_as_float(r5[2][k]) + __uint_as_float(r5[3][k]), gg[1] * (__uint_as_float(r5[0][k]) + __uint_as_float(r5[1][k])));
            auto s0 = __builtin_amdgcn_permlane32_swap(__float_as_uint(t[0]), __float_as_uint(t[2]), false, false);
            auto s1 = __builtin_amdgcn_permlane32_swap(__float_as_uint(t[1]), __float_as_uint(t[3]), false, false);
            const float u0 = __uint_as_float(s0[0]) + __uint_as_float(s0[1]), u1 = __uint_as_float(s1[0]) + __uint_as_float(s1[1]);
            auto s2 = __builtin_amdgcn_permlane16_swap(__float_as_uint(u0), __float_as_uint(u1), false, false);
            float v = __uint_as_float(s2[0]) + __uint_as_float(s2[1]);
            v += dpp<ROR8>(v);
            *(LAS float*)(L + a_rw) = v;
            barrier_lds();
            if (wave == 6) {
                const f32x4 x0 = *(LAS f32x4*)(L + a_rr), x1 = *(LAS f32x4*)(L + a_rr + 16);
                const float res = (((x0[0] + x0[1]) + (x0[2] + x0[3])) + ((x1[0] + x1[1]) + (x1[2] + x1[3]))) + obias;
                __builtin_amdgcn_raw_buffer_store_b16((u16)pk(res, res), __builtin_amdgcn_make_buffer_rsrc(out, 0, 2304, 0x00020000), lane < 32 ? bid * 64 + lane * 2 : NOWHERE, 0, 0);
                st64(ws, tag, bid == 0 && lane == 0 ? WS_E : NOWHERE);
            }
            STAMP(15);
        }
    } else {
        const int sb = slot - 16, chunk = xcd * 8 + (sb >> 1), hh = sb & 1, b = chunk >> 5, bi = hh * 64 + chunk;
        asm volatile("s_nop 4" ::: "memory");
        v2u e = {0u, 0u};
        if (wave == 0) ald_e(e, wsd);
        wait_until(t0 + PF_T);
        if (UK_PF == 1) {
            if (tid < 256) touch(mk_srd(wuk, 512 * 8192), (sb * 32 + (tid >> 3)) * 8192 + xcd * 1024 + (tid & 7) * 128);
            asm volatile("s_barrier" ::: "memory");
        }
        STAMPE(1);
        v4u fa = {0u, 0u, 0u, 0u};
        const v4i srd_c = mk_srd(ckv + (size_t)chunk * 128 * 512, 131072);
#define CKV_ROW(i) do { const int r = (((i) + wave) & 7) + 8 * (((i) >> 3) ^ hh); \
            dma16(srd_c, (16 * lane) ^ (16 * (2 * (r & 7) + (r >> 3))), (16 * wave + r) * 1024, lds0 + LB_CKV + (16 * wave + r) * 1024); } while (0)
#pragma unroll
        for (int i = 0; i < 8; ++i) CKV_ROW(i);
        fence_sched();
        unsigned o_c3 = WS_H3 + chunk * 32768 + hh * 16384 + tid * 16;
        unsigned o_c5 = tid < 288 ? WS_H5 + bid * 4608 + tid * 16 : tid < 296 ? WS_ML + chunk * 256 + hh * 128 + (tid - 288) * 16 : NOWHERE;
#define B_MARKS() do { st128(ws, CLEAN, o_c3); st128(ws, CLEAN, o_c3b); st128(ws, CLEAN, o_c5); } while (0)
        if (tid < 2) *(LAS unsigned*)(L + LB_CNT + 4 * tid) = 0u;
        if (tid < 4) *(LAS unsigned*)(L + LB_ZERO + tid * 4) = 0u;
        const int fsw = 2 * (n & 7) + (n >> 3), pg = ((g & 1) << 1) | (g >> 1), rr = 4 * g + q, fr = 2 * (rr & 7) + (rr >> 3);
        unsigned a_k[4], a_qr[4], a_v[4][2];
#pragma unroll
        for (int s = 0; s < 4; ++s) {
            a_qr[s] = LB_Q + n * 1024 + 16 * ((4 * s + pg) ^ fsw); a_k[s] = a_qr[s] - LB_Q + LB_CKV + wave * 16384;
            a_v[s][0] = LB_CKV + rr * 1024 + 256 * (wave >> 1) + 16 * ((8 * (wave & 1) + 2 * s + (p >> 1)) ^ fr) + 8 * (p & 1); a_v[s][1] = a_v[s][0] + 65536;
            pin(a_qr[s]); pin(a_k[s]); pin(a_v[s][0]); pin(a_v[s][1]);
        }
        unsigned o_qa = WS_QA + b * 32768 + hh * 16384 + tid * 16, a_qw0 = LB_Q + wave * 1024 + 16 * (lane ^ (2 * wave)), a_qw1 = LB_Q + (wave + 8) * 1024 + 16 * (lane ^ (2 * wave + 1));
        unsigned a_mx = LB_MX + n * 32, a_mw = LB_MX + n * 32 + wave * 4, a_pw = LB_P + (wave >> 1) * 1024 + lane * 16 + (wave & 1) * 8, a_pr = LB_P + lane * 16;
        unsigned o_h3 = WS_H3 + chunk * 32768 + (hh * 16 + (n & 7)) * 1024 + wave * 128 + (n >> 3) * 64 + pg * 16;
        unsigned o_p1 = WS_P1 + (hh * 64 + lane) * 128, o_ml = g == 0 ? WS_ML + chunk * 256 + (hh * 16 + n) * 8 : NOWHERE, o_h3b = o_h3 + 8192;
        pin(o_ml); pin(o_h3b);
        pin(o_qa); pin(a_qw0); pin(a_qw1); pin(a_mx); pin(a_mw); pin(a_pw); pin(a_pr); pin(o_h3); pin(o_p1); pin(o_c3); pin(o_c5);
        unsigned o_c3b = o_c3 + 8192; pin(o_c3b);
        const v4i srd_2 = mk_srd(w2 + (size_t)bi * 36 * 576, 36 * 1152);
        unsigned v_w = (lane >> 3) * 1152 + 16 * ((lane & 7) ^ ((((lane >> 4) & 1) | ((wave & 1) << 1)) << 1));
        unsigned a_w = LB_W + g * 9216 + (wave >> 1) * 3072 + q * 128 + 16 * ((4 * (wave & 1) + (p >> 1)) ^ (((q >> 1) | ((g & 1) << 1)) << 1)) + 8 * (p & 1), a_w1 = a_w ^ 32;
        unsigned a_wt = LB_W + 4 * 9216 + (wave >> 1) * 3072 + q * 128 + 16 * ((4 * (wave & 1) + (p >> 1)) ^ ((q >> 1) << 1)) + 8 * (p & 1), a_wt1 = a_wt ^ 32;
        unsigned o_h5 = n < 12 ? WS_H5 + bid * 4608 + (n & 1) * 2304 + ((3 * (wave >> 1) + (n >> 2)) * 64 + (2 * (wave & 1) + ((n >> 1) & 1)) * 16 + 4 * g) * 4 : NOWHERE;
        unsigned a_h = LB_H + (n & 1) * 256 + 16 * g, a_ht = LB_H + (n & 1) * 256 + 64 + 8 * g;
        unsigned o1 = WS_M1 + (bi & ~3) * 1152 + (wave & 1) * 576 + ((bi & 3) * 36 + (lane < 36 ? lane : 35)) * 4;
        pin(v_w); pin(a_w); pin(a_w1); pin(a_wt); pin(a_wt1); pin(o_h5); pin(a_h); pin(a_ht); pin(o1);
        v4u m1 = {0u, 0u, 0u, 0u};
        unsigned b1v = 0;
        if (wave >= 6) asm volatile("buffer_load_ushort %0, %1, %2, 0 offen" : "+v"(b1v) : "v"(lane * 2), "s"(mk_srd(b1 + bi * 36, 72)) : "memory");
        constexpr int NG = 9 - 2 * WO_B > 0 ? 9 - 2 * WO_B : 0;
        unsigned o_pfo = NOWHERE;
        if (BPF != 0 && NG != 0) { const int t = tid < 32 * NG ? tid : 0, r = t / (NG ? NG : 1), k = t % (NG ? NG : 1); o_pfo = tid < 32 * NG ? (xcd * 512 + sb * 32 + r) * 1152 + (k < 5 - WO_B ? WO_B + k : 2 * WO_B + k) * 128 : NOWHERE; }
        pin(o_pfo);
        pin(m1);
        asm volatile("s_barrier" ::: "memory");
        v2u tag = {0u, 0u};
        u64 t_q = 0;
        bool late = false;
#pragma unroll
        for (int i = 8; i < 16; ++i) {
            if (i == FA_AT && wave == 0) asm volatile("buffer_load_dwordx4 %0, %1, %2, 0 offen sc1" : "+v"(fa) : "v"(o_p1), "s"(wsd) : "memory");
            if (i == FA_USE && wave == 0) {
                asm volatile("s_waitcnt vmcnt(%2)" : "+v"(fa), "+v"(e) : "i"(FA_USE - FA_AT) : "memory");
                tag = mk_tag(e);
                late = !flags_ok((v2u){fa.x, fa.y}, tag);
                int d = (int)(fa.z - (unsigned)t0);
                d = max(d, __builtin_amdgcn_mov_dpp(d, XOR1, 0xf, 0xf, true)); d = max(d, __builtin_amdgcn_mov_dpp(d, XOR2, 0xf, 0xf, true));
                d = max(d, __builtin_amdgcn_mov_dpp(d, ROR4, 0xf, 0xf, true)); d = max(d, __builtin_amdgcn_mov_dpp(d, ROR8, 0xf, 0xf, true));
                d = max(max(__builtin_amdgcn_readlane(d, 0), __builtin_amdgcn_readlane(d, 16)), max(__builtin_amdgcn_readlane(d, 32), __builtin_amdgcn_readlane(d, 48)));
                t_q = t0 + min(max(d, 0), QA_T + 200);
                asm volatile("s_nop 4" ::: "memory");
            }
            CKV_ROW(i);
        }
        STAMPE(2);
        if (wave == 0) {
            if (__builtin_expect(late, 0)) {
                wait_flags<4>(ws, o_p1, tag);
                wait_flags<0>(ws, o_p1 + (WS_P2 - WS_P1), tag);
            } else wait_until(t_q);
        }
        if (Q_DELAY && wave == 0) __builtin_amdgcn_s_sleep(Q_DELAY);
        asm volatile("s_barrier" ::: "memory");
        STAMPE(3);
        v4u kf[16];
        {
            v4u r[2];
            const unsigned off[2] = {o_qa, o_qa + 8192};
            r[0] = ald128(wsd, off[0]); r[1] = ald128(wsd, off[1]);
            asm volatile("s_barrier" ::: "memory"); B_MARKS();
            WAIT_VM(5, "+v"(b1v));
            STAMP(12);
#pragma unroll
            for (int s = 0; s < 16; ++s) kf[s] = lds128(L, a_k[s & 3] + 256 * (s >> 2));
            asm volatile("s_waitcnt vmcnt(%2)" : "+v"(r[0]), "+v"(r[1]) : "i"(3) : "memory");
            STAMP(13);
            if (__builtin_expect(!landed<2>(r), 0)) regather<2, Q_SLP>(ws, off, r);
            *(LAS v4u*)(L + a_qw0) = r[0]; *(LAS v4u*)(L + a_qw1) = r[1];
        }
        STAMP(4);
        barrier_lds();
        STAMP(5);
        fence_sched();
        if (BPF == 2 && NG != 0 && wave * 64 < 32 * NG) touch(mk_srd(wo, 4096 * 1152), o_pfo);
        if (W_PF && tid < W_PF)
            touch(mk_srd(w2 + (size_t)bi * 36 * 576, 36 * 1152), tid * 128);
        f32x4 sc = {0.f, 0.f, 0.f, 0.f};
        v4u vf[16];
#define V_FRAG(i) vf[i] = lds_tr2(L, a_v[(i) >> 2][((i) >> 1) & 1] + ((i) & 1) * 32768, a_v[(i) >> 2][((i) >> 1) & 1] + ((i) & 1) * 32768 + 16384)
        {
            v4u qf[16];
#pragma unroll
            for (int s = 0; s < 16; ++s) qf[s] = lds128(L, a_qr[s & 3] + 256 * (s >> 2));
            fence_sched();
#pragma unroll
            for (int s = 0; s < 8; ++s) sc = mfma32(kf[s], qf[s], sc);
            fence_sched();
#pragma unroll
            for (int i = 0; i < TR_QK; ++i) V_FRAG(i);
            fence_sched();
#pragma unroll
            for (int s = 8; s < 16; ++s) sc = mfma32(kf[s], qf[s], sc);
        }
        *(LAS float*)(L + a_mw) = x32max(x16max(fmx(fmx(sc[0], sc[1]), fmx(sc[2], sc[3]))));
        if (R3_AT == 2) {
            if ((W_PF && wave * 64 < W_PF) || (BPF == 2 && wave * 64 < 32 * NG)) wait_vm<1>(); else wait_vm<0>();
        }
        barrier_lds();
        if (R3_AT == 2 && tid == 0) st64(ws, tag, WS_R3 + bi * 128);
        STAMP(6);
        float mx;
        {
            const f32x4 m0 = *(LAS f32x4*)(L + a_mx), m1 = *(LAS f32x4*)(L + a_mx + 16);
            fence_sched();
#pragma unroll
            for (int i = TR_QK; i < TR_EARLY; ++i) V_FRAG(i);
            fence_sched();
            mx = fmx(fmx(fmx(m0[0], m0[1]), fmx(m0[2], m0[3])), fmx(fmx(m1[0], m1[1]), fmx(m1[2], m1[3])));
            const float p0 = __builtin_amdgcn_exp2f(sc[0] - mx), p1 = __builtin_amdgcn_exp2f(sc[1] - mx), p2 = __builtin_amdgcn_exp2f(sc[2] - mx), p3 = __builtin_amdgcn_exp2f(sc[3] - mx);
            *(LAS v2u*)(L + a_pw) = (v2u){pk(p0, p1), pk(p2, p3)};
            *(LAS float*)(L + a_mw + (LB_L - LB_MX)) = x32sum(x16sum((p0 + p1) + (p2 + p3)));
        }
        if (R3_AT == 3) {
            if ((W_PF && wave * 64 < W_PF) || (BPF == 2 && wave * 64 < 32 * NG)) wait_vm<1>(); else wait_vm<0>();
        }
        barrier_lds();
        if (R3_AT == 3 && tid == 0) st64(ws, tag, WS_R3 + bi * 128);
        STAMP(7);
        {
            if (PV_PRIO && wave < 4) __builtin_amdgcn_s_setprio(1);
            v4u pf[4];
#pragma unroll
            for (int s = 0; s < 4; ++s) pf[s] = lds128(L, a_pr + s * 1024);
            const f32x4 l0 = *(LAS f32x4*)(L + a_mx + (LB_L - LB_MX)), l1 = *(LAS f32x4*)(L + a_mx + (LB_L - LB_MX) + 16);
            fence_sched();
#pragma unroll
            for (int i = TR_EARLY; i < 16; ++i) V_FRAG(i);
            fence_sched();
            if (wave == 7) st64(ws, (v2u){nos(__float_as_uint(mx)), nos(__float_as_uint(((l0[0] + l0[1]) + (l0[2] + l0[3])) + ((l1[0] + l1[1]) + (l1[2] + l1[3]))))}, o_ml);
            v4u o[2];
#pragma unroll
            for (int u = 0; u < 2; ++u) {
                f32x4 c0 = {0.f, 0.f, 0.f, 0.f}, c1 = {0.f, 0.f, 0.f, 0.f};
#pragma unroll
                for (int s = 0; s < 4; ++s) c0 = mfma32(vf[8 * u + s], pf[s], c0);
#pragma unroll
                for (int s = 0; s < 4; ++s) c1 = mfma32(vf[8 * u + 4 + s], pf[s], c1);
                auto s0 = __builtin_amdgcn_permlane16_swap(nos(pk(c0[0], c0[1])), nos(pk(c1[0], c1[1])), false, false);
                auto s1 = __builtin_amdgcn_permlane16_swap(nos(pk(c0[2], c0[3])), nos(pk(c1[2], c1[3])), false, false);
                o[u] = (v4u){s0[0], s1[0], s0[1], s1[1]};
            }
            v4u x0, x1;
#pragma unroll
            for (int k = 0; k < 4; ++k) {
                x0[k] = __builtin_amdgcn_update_dpp(o[0][k], o[1][k], ROR8, 0xf, 0xc, false);
                x1[k] = __builtin_amdgcn_update_dpp(o[1][k], o[0][k], ROR8, 0xf, 0x3, false);
            }
            st128(ws, x0, o_h3); st128(ws, x1, o_h3b);
            if (PV_PRIO) __builtin_amdgcn_s_setprio(0);
        }
        STAMP(8);
        if (wave >= 6) {
#pragma unroll
            for (int k = 0; k < 4; ++k) asm volatile("buffer_load_dword %0, %1, %2, 0 offen offset:%3 sc1" : "+v"(m1[k]) : "v"(o1), "s"(wsd), "i"(k * 1152) : "memory");
        }
        barrier_lds();
        if (BPF == 1 && NG != 0) { touch(mk_srd(wo, 4096 * 1152), o_pfo); asm volatile("s_barrier" ::: "memory"); }
        if (wave < 6) {
            if (W_DELAY) __builtin_amdgcn_s_sleep(W_DELAY);
            const int third = wave >> 1;
            const unsigned so = (wave & 1) * 9216 + third * 384, lo = lds0 + LB_W + (wave & 1) * 9216 + third * 3072;
            if (!(wave & 1)) {
#pragma unroll
                for (int i = 0; i < 3; ++i) dma16w(srd_2, v_w, so + 4 * 9216 + i * 128, lo + 4 * 9216 + i * 1024);
            }
#pragma unroll
            for (int i = 0; i < 6; ++i) dma16w(srd_2, v_w, so + (i / 3) * 18432 + (i % 3) * 128, lo + (i / 3) * 18432 + (i % 3) * 1024);
            wait_vm<0>();
            lds_arrive(L, LB_CNT, lane);
            lds_wait(L, LB_CNT, 8);
            STAMP(9);
            v4u wm[6]; v2u wt[6];
            const v4u f1 = lds128(L, a_h);
            const v2u ft = *(LAS v2u*)(L + a_ht);
#pragma unroll
            for (int i = 0; i < 6; ++i) {
                const unsigned aw = (i & 1) ? a_w1 : a_w, at = (i & 1) ? a_wt1 : a_wt, o = (i >> 1) * 1024;
                wm[i] = lds_tr2(L, aw + o, aw + o + 512); wt[i] = lds_tr(L, at + o);
            }
            fence_sched();
            f32x4 sel;
#pragma unroll
            for (int i = 0; i < 6; ++i) {
                const f32x4 c = mfma16(wt[i], ft, mfma32(wm[i], f1, (f32x4){0.f, 0.f, 0.f, 0.f}));
#pragma unroll
                for (int k = 0; k < 4; ++k) sel[k] = (i == 0 || (n >> 1) == i) ? c[k] : sel[k];
            }
            st128(ws, (v4u){nos(__float_as_uint(sel[0])), nos(__float_as_uint(sel[1])), nos(__float_as_uint(sel[2])), nos(__float_as_uint(sel[3]))}, o_h5);
        } else {
            WAIT_VM(0, "+v"(m1));
#pragma unroll 1
            for (unsigned spins = 0; spins < SPIN_CAP && __any(umax4(m1) == SENT); ++spins) {
                asm volatile("" ::: "memory");
                m1 = (v4u){ld32(ws, o1), ld32(ws, o1 + 1152), ld32(ws, o1 + 2304), ld32(ws, o1 + 3456)};
            }
            const float v = bfr(bfr((__uint_as_float(m1.x) + __uint_as_float(m1.y)) + (__uint_as_float(m1.z) + __uint_as_float(m1.w))) + bflo(b1v));
            const float u = 0.7978845608028654f * (v + 0.044715f * v * v * v);
            const float hv = v / (1.f + __expf(-2.f * u));
            if (lane < 48) *(LAS u16*)(L + LB_H + (wave & 1) * 256 + lane * 2) = lane < 36 ? (u16)pk(hv, hv) : (u16)0;
            asm volatile("s_waitcnt lgkmcnt(0)" ::: "memory");
            lds_arrive(L, LB_CNT, lane);
            STAMP(14);
        }
        STAMP(11);
    }
    if (lane < 16) __hip_atomic_store((GAS u64*)(wsp + WS_DBG) + (bid * 8 + wave) * 16 + lane, (u64)stampv, __ATOMIC_RELAXED, __HIP_MEMORY_SCOPE_AGENT);
}

template <int MODE, int ROLE, int IDX>
__global__ void spin_kernel(char* wsp) {
    u64 tin64; asm volatile("s_memrealtime %0\n\ts_waitcnt lgkmcnt(0)" : "=s"(tin64) :: "memory");
    const unsigned tin = (unsigned)tin64;
    __shared__ unsigned sm[2][256];
    const GAS u64* d = (const GAS u64*)(wsp + WS_DBG);
    unsigned a0 = 0, v = 0xFFFFFFFFu;
    const int bid = threadIdx.x;
    for (int w = 0; w < 8; ++w) {
        const GAS u64* s = d + (bid * 8 + w) * 16;
        unsigned age = tin - (unsigned)s[0]; a0 = age > a0 ? age : a0;
        for (int i = 0; i < 16; ++i) if (MODE != 3 || (i == IDX && ((bid >> 3) >= 16) == ROLE)) { unsigned x = (unsigned)s[i]; if (x) { age = tin - x; v = age < v ? age : v; } }
    }
    sm[0][bid] = a0; sm[1][bid] = v;
    __syncthreads();
    if (bid == 0) {
        for (int i = 0; i < 256; ++i) { a0 = sm[0][i] > a0 ? sm[0][i] : a0; v = sm[1][i] < v ? sm[1][i] : v; }
        u64 span = MODE == 0 ? v : MODE == 2 ? 1 : a0 - v;
        if (span > 5000) span = 5000;
        u64 t, t1; asm volatile("s_memrealtime %0\n\ts_waitcnt lgkmcnt(0)" : "=s"(t) :: "memory");
        do { asm volatile("s_memrealtime %0\n\ts_waitcnt lgkmcnt(0)" : "=s"(t1) :: "memory"); } while (t1 < t + span * 100);
    }
}
extern "C" void kernel_launch(void* const* d_in, const int* in_sizes, int n_in, void* d_out, int out_size, void* d_ws, size_t ws_size, hipStream_t stream) {
    fused_decode_kernel<<<256, NT, 0, stream>>>((const u16*)d_in[0], (const u16*)d_in[1], (const u16*)d_in[2], (const u16*)d_in[3], (const u16*)d_in[4],
                                                (const u16*)d_in[5], (const u16*)d_in[7], (const u16*)d_in[8], (const u16*)d_in[9], (const u16*)d_in[10],
                                                (const u16*)d_in[11], (u16*)d_out, (char*)d_ws);
    spin_kernel<3, 1, 0><<<1, 256, 0, stream>>>((char*)d_ws);
}
```

```cpp
#include <hip/hip_runtime.h>
#include <stdint.h>

typedef unsigned short u16;
typedef unsigned long long u64;
typedef unsigned v4u __attribute__((ext_vector_type(4)));
typedef unsigned v2u __attribute__((ext_vector_type(2)));
typedef int v4i __attribute__((ext_vector_type(4)));
typedef float f32x4 __attribute__((ext_vector_type(4)));
typedef float f32x2 __attribute__((ext_vector_type(2)));
typedef short s16x4 __attribute__((ext_vector_type(4)));
typedef __bf16 bf16x8 __attribute__((ext_vector_type(8)));
typedef __bf16 bf16x2 __attribute__((ext_vector_type(2)));
#define GAS __attribute__((address_space(1)))
#define LAS __attribute__((address_space(3)))
#define DI __device__ __forceinline__
typedef __amdgpu_buffer_rsrc_t rsrc_t;

constexpr int NT = 512;
constexpr unsigned SENT = 0xFFFFFFFFu;
constexpr int SC1 = 16;
constexpr unsigned SPIN_CAP = 1u << 17;
constexpr u64 TAG_STEP = 0x9E3779B97F4A7C15ull;

constexpr int WS_E = 0;
constexpr int WS_P1 = 4096;
constexpr int WS_R3 = WS_P1 + 16384;
constexpr int WS_P2 = WS_R3 + 16384;
constexpr int WS_H1 = WS_P2 + 16384;
constexpr int WS_QA = WS_H1 + 128 * 1024;
constexpr int WS_M1 = WS_QA + 65536;
constexpr int WS_ML = WS_M1 + 128 * 1152;
constexpr int WS_H3 = WS_ML + 16384;
constexpr int WS_H5 = WS_H3 + 64 * 32768;
constexpr int WS_END = WS_H5 + 256 * 4608;
constexpr unsigned NOWHERE = 0x7FFFFF00u;

constexpr int LA_WQ = 0, LA_WUK = 36864, LA_WO = 0, LA_W1 = 73728, LA_WUV = 115200, LA_QF = 147968, LA_CMB = LA_QF, LA_COL = 156160, LA_TAG = 156288, LA_G = 156304, LA_ZERO = 156320  ,
              LA_X = 156416, LA_RED = 157056, LA_CTX = 158080;
constexpr int LB_CKV = 0, LB_W = 0, LB_Q = 131072, LB_P = 147456, LB_MX = 151552, LB_L = 152064, LB_H = 152576, LB_CNT = 153088;
constexpr int LDS_BYTES = 158464;
constexpr int CODE_LINES = 148;

DI v4i mk_srd(const void* p, unsigned bytes) {
    const u64 a = (u64)p;
    return (v4i){(int)(unsigned)a, (int)((unsigned)(a >> 32) & 0xFFFFu), (int)bytes, 0x00020000};
}
DI void dma16(v4i srd, unsigned voff, unsigned soff, unsigned lds) {
    asm volatile("s_mov_b32 m0, %0\n\ts_nop 0\n\tbuffer_load_dwordx4 %1, %2, %3 offen lds" :: "s"(lds), "v"(voff), "s"(srd), "s"(soff) : "memory");
}
DI void touch(v4i srd, unsigned voff) { asm volatile("buffer_load_dword v255, %0, %1, 0 offen" :: "v"(voff), "s"(srd) : "memory", "v255"); }
DI u64 now() { u64 t; asm volatile("s_memrealtime %0\n\ts_waitcnt lgkmcnt(0)" : "=s"(t) :: "memory"); return t; }
DI void wait_until(u64 t) { while (now() < t) __builtin_amdgcn_s_sleep(2); }
DI v4u ald128(v4i srd, unsigned voff) { v4u r; asm volatile("buffer_load_dwordx4 %0, %1, %2, 0 offen sc1" : "=v"(r) : "v"(voff), "s"(srd) : "memory"); return r; }
DI void ald_e(v2u& r, v4i srd) { asm volatile("buffer_load_dwordx2 %0, off, %1, 0 sc1" : "+v"(r) : "s"(srd) : "memory"); }
#define WAIT_VM(N, ...) asm volatile("s_waitcnt vmcnt(" #N ")" : __VA_ARGS__ :: "memory")
DI bool flags_ok(v2u f, v2u tag) { return __all((f.x == tag.x) & (f.y == tag.y)); }
DI v2u mk_tag(v2u e) { const u64 t = (((u64)e.y << 32) | e.x) + TAG_STEP; return (v2u){(unsigned)t, (unsigned)(t >> 32)}; }
template <class T> DI void pin(T& v) { asm volatile("" : "+v"(v)); }
DI void fence_sched() { __builtin_amdgcn_sched_barrier(0); }
template <int N> DI void wait_vm() { asm volatile("s_waitcnt vmcnt(%0)" :: "i"(N) : "memory"); }
DI void barrier_lds() { asm volatile("s_waitcnt lgkmcnt(0)\n\ts_barrier" ::: "memory"); }
DI f32x4 mfma32(v4u a, v4u b, f32x4 c) { return __builtin_amdgcn_mfma_f32_16x16x32_bf16(__builtin_bit_cast(bf16x8, a), __builtin_bit_cast(bf16x8, b), c, 0, 0, 0); }
DI f32x4 mfma16(v2u a, v2u b, f32x4 c) { return mfma32((v4u){a.x, a.y, 0u, 0u}, (v4u){b.x, b.y, 0u, 0u}, c); }
DI v2u lds_tr(LAS char* L, unsigned off) { return __builtin_bit_cast(v2u, __builtin_amdgcn_ds_read_tr16_b64_v4i16((LAS s16x4*)(L + off))); }
DI v4u lds_tr2(LAS char* L, unsigned o0, unsigned o1) { const v2u a = lds_tr(L, o0), b = lds_tr(L, o1); return (v4u){a.x, a.y, b.x, b.y}; }
DI v4u lds128(LAS char* L, unsigned off) { return *(LAS v4u*)(L + off); }
DI unsigned pk(float lo, float hi) { return __builtin_bit_cast(unsigned, __builtin_convertvector((f32x2){lo, hi}, bf16x2)); }
DI float bflo(unsigned v) { return __uint_as_float(v << 16); }
DI float bfhi(unsigned v) { return __uint_as_float(v & 0xFFFF0000u); }
DI float bfr(float f) { return bflo(pk(f, f)); }
template <int CTRL> DI float dpp(float x) { return __builtin_bit_cast(float, __builtin_amdgcn_mov_dpp(__builtin_bit_cast(int, x), CTRL, 0xf, 0xf, true)); }
DI float fmx(float a, float b) { return __builtin_elementwise_maximum(a, b); }
constexpr int XOR1 = 0xB1, XOR2 = 0x4E, ROR4 = 0x124, ROR8 = 0x128;
DI float x16max(float x) { auto s = __builtin_amdgcn_permlane16_swap(__float_as_uint(x), __float_as_uint(x), false, false); return fmx(__uint_as_float(s[0]), __uint_as_float(s[1])); }
DI float x32max(float x) { auto s = __builtin_amdgcn_permlane32_swap(__float_as_uint(x), __float_as_uint(x), false, false); return fmx(__uint_as_float(s[0]), __uint_as_float(s[1])); }
DI float x16sum(float x) { auto s = __builtin_amdgcn_permlane16_swap(__float_as_uint(x), __float_as_uint(x), false, false); return __uint_as_float(s[0]) + __uint_as_float(s[1]); }
DI float x32sum(float x) { auto s = __builtin_amdgcn_permlane32_swap(__float_as_uint(x), __float_as_uint(x), false, false); return __uint_as_float(s[0]) + __uint_as_float(s[1]); }
DI unsigned umax2(unsigned a, unsigned b) { return a > b ? a : b; }
DI unsigned umax4(v4u v) { return umax2(umax2(v.x, v.y), umax2(v.z, v.w)); }
DI void st128(rsrc_t rs, v4u v, unsigned off) { __builtin_amdgcn_raw_buffer_store_b128(v, rs, off, 0, SC1); }
DI void st64(rsrc_t rs, v2u v, unsigned off) { __builtin_amdgcn_raw_buffer_store_b64(v, rs, off, 0, SC1); }
DI v4u ld128(rsrc_t rs, unsigned off) { return __builtin_amdgcn_raw_buffer_load_b128(rs, off, 0, SC1); }
DI v2u ld64(rsrc_t rs, unsigned off) { return __builtin_amdgcn_raw_buffer_load_b64(rs, off, 0, SC1); }
DI unsigned ld32(rsrc_t rs, unsigned off) { return __builtin_amdgcn_raw_buffer_load_b32(rs, off, 0, SC1); }

template <int SLP> DI void wait_flags(rsrc_t rs, unsigned off, v2u tag) {
#pragma unroll 1
    for (unsigned spins = 0; spins < SPIN_CAP; ++spins) {
        asm volatile("" ::: "memory");
        const v2u f = ld64(rs, off);
        if (flags_ok(f, tag)) break;
        if (SLP) __builtin_amdgcn_s_sleep(SLP);
    }
    asm volatile("" ::: "memory");
}
template <int GAP> DI void wait_flags3(v4i srd, unsigned off, v2u want) {
    unsigned cnt;
#define LOOK(R) "buffer_load_dwordx2 " R ", %1, %2, 0 offen sc1\n\t"
#define SEEN(R) "s_waitcnt vmcnt(2)\n\tv_cmp_eq_u64_e32 vcc, " R ", %3\n\ts_cmp_eq_u64 vcc, exec\n\ts_cbranch_scc1 2f\n\t"
    asm volatile(LOOK("v[248:249]") "s_sleep %5\n\t" LOOK("v[250:251]") "s_sleep %5\n\ts_mov_b32 %0, %4\n"
                 "1:\n\t" LOOK("v[252:253]") SEEN("v[248:249]") LOOK("v[248:249]") SEEN("v[250:251]") LOOK("v[250:251]") SEEN("v[252:253]")
                 "s_sub_u32 %0, %0, 1\n\ts_cbranch_scc0 1b\n"
                 "2:"
                 : "=&s"(cnt) : "v"(off), "s"(srd), "v"(want), "i"(SPIN_CAP), "i"(GAP)
                 : "memory", "vcc", "scc", "v248", "v249", "v250", "v251", "v252", "v253");
#undef LOOK
#undef SEEN
}
template <int N> DI bool landed(const v4u (&r)[N]) {
    unsigned m = 0;
#pragma unroll
    for (int i = 0; i < N; ++i) m = umax2(m, umax4(r[i]));
    return !__any(m == SENT);
}
template <int N, int SLP = 0> DI void regather(rsrc_t rs, const unsigned (&off)[N], v4u (&r)[N]) {
#pragma unroll 1
    for (unsigned spins = 0; spins < SPIN_CAP; ++spins) {
        if (SLP) __builtin_amdgcn_s_sleep(SLP);
        asm volatile("" ::: "memory");
#pragma unroll
        for (int i = 0; i < N; ++i) {
            const bool miss = umax4(r[i]) == SENT;
            const v4u v = ld128(rs, miss ? off[i] : NOWHERE);
            r[i] = miss ? v : r[i];
        }
        if (landed<N>(r)) break;
    }
}

DI void lds_arrive(LAS char* L, int off, int lane) { if (lane == 0) __hip_atomic_fetch_add((LAS unsigned*)(L + off), 1u, __ATOMIC_RELAXED, __HIP_MEMORY_SCOPE_WORKGROUP); }
DI void lds_wait(LAS char* L, int off, unsigned count) {
#pragma unroll 1
    for (unsigned spins = 0; spins < (SPIN_CAP << 4); ++spins) {
        asm volatile("" ::: "memory");
        if (__builtin_amdgcn_readfirstlane(*(volatile LAS unsigned*)(L + off)) >= count) break;
    }
    asm volatile("" ::: "memory");
}
DI v4u tagged4(f32x4 c, v2u tag) {
    const unsigned p01 = pk(c[0], c[1]), p23 = pk(c[2], c[3]);
    return (v4u){__builtin_amdgcn_perm(p01, tag.x, 0x05040100), __builtin_amdgcn_perm(p01, tag.x, 0x07060302),
                 __builtin_amdgcn_perm(p23, tag.y, 0x05040100), __builtin_amdgcn_perm(p23, tag.y, 0x07060302)};
}
DI unsigned tag_miss(v4u t, v2u tag) { return (__builtin_amdgcn_perm(t.y, t.x, 0x05040100) ^ tag.x) | (__builtin_amdgcn_perm(t.w, t.z, 0x05040100) ^ tag.y); }
DI float gate_of(const v4u (&xv)[2], const v4u (&gv)[2]) {
    float s = 0.f;
#pragma unroll
    for (int i = 0; i < 2; ++i)
#pragma unroll
        for (int k = 0; k < 4; ++k) { const unsigned pr = pk(bflo(xv[i][k]) * bflo(gv[i][k]), bfhi(xv[i][k]) * bfhi(gv[i][k])); s += bflo(pr) + bfhi(pr); }
    s += dpp<XOR1>(s); s += dpp<XOR2>(s); s += dpp<ROR4>(s); s += dpp<ROR8>(s); s = x32sum(x16sum(s));
    return bfr(1.f / (1.f + __expf(-bfr(s))));
}

constexpr int PF_T = 100;
constexpr int H1_T = 208;
constexpr int QA_T = 342;
constexpr int A_T = 315;
constexpr int H1_DELAY = 8;
constexpr int W_DELAY = 14;
constexpr int H5_DELAY = 4;
constexpr int FA_AT = 9;
constexpr int FA_USE = 14;
constexpr int TR_EARLY = 6;
constexpr int WO_A = 2;
constexpr int WO_B = 4;

__global__ void __launch_bounds__(NT, 2) __attribute__((amdgpu_waves_per_eu(2, 2)))
fused_decode_kernel(const u16* __restrict__ xp, const u16* __restrict__ ckv, const u16* __restrict__ wq, const u16* __restrict__ wuk,
                    const u16* __restrict__ wuv, const u16* __restrict__ wo, const u16* __restrict__ w1, const u16* __restrict__ b1,
                    const u16* __restrict__ w2, const u16* __restrict__ b2, const u16* __restrict__ gw, u16* __restrict__ out, char* wsp) {
    __shared__ __attribute__((aligned(1024))) char smem[LDS_BYTES];
    LAS char* L = (LAS char*)smem;
    const unsigned lds0 = (unsigned)(size_t)L;
    const int bid = blockIdx.x, tid = threadIdx.x, lane = tid & 63;
    const int xcd = bid & 7, slot = bid >> 3;
    u64 pc0; asm volatile("s_getpc_b64 %0" : "=s"(pc0));
    if (slot >= 16 && slot - 16 + 16 * tid < CODE_LINES)
        asm volatile("global_load_dword v255, %0, %1" :: "v"((slot - 16 + 16 * tid) * 128), "s"(pc0 & ~127ull) : "memory", "v255");
    const u64 t0 = now();
    asm volatile("" :: "s"(xp), "s"(ckv), "s"(wq), "s"(wuk), "s"(wuv), "s"(wo), "s"(w1), "s"(b1), "s"(w2), "s"(b2), "s"(gw), "s"(out), "s"(wsp));
    const int wave = __builtin_amdgcn_readfirstlane(tid >> 6);
    const int g = lane >> 4, n = lane & 15, q = (lane >> 2) & 3, p = lane & 3;
    const rsrc_t ws = __builtin_amdgcn_make_buffer_rsrc(wsp, 0, WS_END, 0x00020000);
    const v4i wsd = mk_srd(wsp, WS_END);
    const v4u CLEAN = {SENT, SENT, SENT, SENT};

    if (__builtin_expect(slot < 16, 1)) {
        const int a = xcd * 16 + slot, h = a >> 2, j = a & 3;
        asm volatile("s_nop 4" ::: "memory");
        const unsigned v_t = g * 8192 + 16 * (n ^ ((4 * ((wave >> 1) & 1) + g) << 1));
        {
            const v4i srd = mk_srd(wq + (size_t)j * 144 * 4096 + h * 128, 143 * 8192 + 256);
#pragma unroll
            for (int i = 0; i < 5; ++i)
                if (i < 4 || wave < 4) dma16(srd, v_t, (wave + 8 * i) * 32768, lds0 + LA_WQ + (wave + 8 * i) * 1024);
        }
        v2u e = {0u, 0u};
        if (wave == 7) {
            ald_e(e, wsd);
            if (lane < 36) dma16(mk_srd(xp, 2304), (lane >= 18 ? 1152 - 288 : 0) + j * 288 + lane * 16, 0, lds0 + LA_X);
            if (lane < 32) st128(ws, CLEAN, WS_QA + g * 32768 + h * 1024 + j * 256 + n * 16);
        }
        v4u xv[2] = {{0u, 0u, 0u, 0u}, {0u, 0u, 0u, 0u}}, gv[2] = {{0u, 0u, 0u, 0u}, {0u, 0u, 0u, 0u}};
        unsigned b2v = 0;
        if (wave == 6 && bid < 36) {
            const v4i sx = mk_srd(xp + (bid / 18) * 576, 1152), sg = mk_srd(gw, 1152);
#pragma unroll
            for (int k = 0; k < 2; ++k) {
                asm volatile("buffer_load_dwordx4 %0, %1, %2, 0 offen offset:%3" : "+v"(xv[k]) : "v"(lane * 16), "s"(sx), "i"(k * 1024) : "memory");
                asm volatile("buffer_load_dwordx4 %0, %1, %2, 0 offen offset:%3" : "+v"(gv[k]) : "v"(lane * 16), "s"(sg), "i"(k * 1024) : "memory");
            }
            asm volatile("buffer_load_ushort %0, %1, %2, 0 offen" : "+v"(b2v) : "v"((lane & 31) * 2), "s"(mk_srd(b2 + bid * 32 - (bid / 18) * 576, 64)) : "memory");
        }
        const v4i srd_uk = mk_srd(wuk + (size_t)j * 128 * 4096 + h * 128, 127 * 8192 + 256);
        unsigned v_uk[4], a_uk[4];
#pragma unroll
        for (int i = 0; i < 4; ++i) {
            v_uk[i] = g * 8192 + 16 * (n ^ (4 * i + g)); a_uk[i] = LA_WUK + (16 * wave + n) * 256 + 16 * ((4 * i + g) ^ n);
            pin(v_uk[i]); pin(a_uk[i]);
        }
#define UK_ROWS() do { _Pragma("unroll") for (int i = 0; i < 4; ++i) dma16(srd_uk, v_uk[i], (16 * wave + 4 * i) * 8192, lds0 + LA_WUK + (16 * wave + 4 * i) * 256); } while (0)
        unsigned a_x = LA_X + (n & 1) * 288 + 16 * g, a_xt = LA_X + (n & 1) * 288 + 256 + 8 * g;
        unsigned a_q = (8 * g + q) * 256 + 16 * ((2 * wave + (p >> 1)) ^ ((q + 4 * (g & 1)) << 1)) + 8 * (p & 1);
        unsigned a_qt = LA_WQ + (128 + 4 * g + q) * 256 + 16 * ((2 * wave + (p >> 1)) ^ ((q + 4 * (g >> 1)) << 1)) + 8 * (p & 1);
        unsigned o_h1 = n < 2 ? WS_H1 + a * 1024 + wave * 128 + n * 64 + g * 16 : NOWHERE;
        unsigned o_g1 = WS_H1 + (4 * h + ((n >> 1) & 3)) * 1024 + (wave & 1) * 512 + (g >> 1) * 128 + (n & 1) * 64 + (g & 1) * 32;
        unsigned o_qa = n < 2 ? WS_QA + n * 32768 + h * 1024 + (128 * j + 16 * wave + 4 * g) * 2 : NOWHERE;
        const v4i srd_w1 = mk_srd(w1 + (size_t)j * 144 * 4608 + h * 144, 143 * 9216 + 288);
        unsigned v_w1 = (lane / 18) * 9216 + (lane % 18) * 16;
        pin(v_w1);
        unsigned a_l16 = LA_QF + lane * 16;
        unsigned o_cl = tid < 200 ? WS_M1 + a * 1152 + (tid - 128) * 16 : tid < 488 ? WS_H5 + bid * 4608 + (tid - 200) * 16 : NOWHERE;
        pin(o_cl);
        pin(a_x); pin(a_xt); pin(a_q); pin(a_qt); pin(o_h1); pin(o_g1); pin(o_qa); pin(a_l16);
        WAIT_VM(0, "+v"(e), "+v"(xv[0]), "+v"(xv[1]), "+v"(gv[0]), "+v"(gv[1]), "+v"(b2v));
        if (tid == 448) *(LAS v2u*)(L + LA_TAG) = mk_tag(e);
        barrier_lds();
        const v2u tag = *(LAS v2u*)(L + LA_TAG);
        v4u xf[4];
#pragma unroll
        for (int s = 0; s < 4; ++s) xf[s] = lds128(L, a_x + 64 * s);
        const v2u xt = *(LAS v2u*)(L + a_xt);
        {
            v4u wf[4];
#pragma unroll
            for (int s = 0; s < 4; ++s) wf[s] = lds_tr2(L, LA_WQ + a_q + s * 8192, LA_WQ + a_q + s * 8192 + 1024);
            const v2u wt = lds_tr(L, a_qt);
            fence_sched();
            f32x4 c = {0.f, 0.f, 0.f, 0.f};
#pragma unroll
            for (int s = 0; s < 4; ++s) c = mfma32(wf[s], xf[s], c);
            c = mfma16(wt, xt, c);
            st128(ws, tagged4(c, tag), o_h1);
        }
        if (wave >= 2) {
            if (tid == 448) st128(ws, (v4u){tag.x, tag.y, (unsigned)t0 + QA_T, 0u}, WS_P1 + a * 128);
            st128(ws, CLEAN, o_cl);
        }
        if (wave < 2) {
            if (H1_DELAY) __builtin_amdgcn_s_sleep(H1_DELAY);
            wait_until(t0 + H1_T);
            v4u t[4];
#pragma unroll
            for (int s = 0; s < 4; ++s) asm volatile("buffer_load_dwordx4 %0, %1, %2, 0 offen offset:%3 sc1" : "=v"(t[s]) : "v"(o_g1), "s"(wsd), "i"((s >> 1) * 256 + (s & 1) * 16) : "memory");
            asm volatile("s_barrier" ::: "memory");
            UK_ROWS();
            WAIT_VM(4, "+v"(t[0]), "+v"(t[1]), "+v"(t[2]), "+v"(t[3]));
            if (__builtin_expect(__any((tag_miss(t[0], tag) | tag_miss(t[1], tag) | tag_miss(t[2], tag) | tag_miss(t[3], tag)) != 0), 0)) {
#pragma unroll 1
                for (unsigned spins = 0; spins < SPIN_CAP; ++spins) {
                    asm volatile("" ::: "memory");
                    unsigned bad = 0;
#pragma unroll
                    for (int s = 0; s < 4; ++s) {
                        const bool miss = tag_miss(t[s], tag) != 0;
                        const v4u v = ld128(ws, miss ? o_g1 + (s >> 1) * 256 + (s & 1) * 16 : NOWHERE);
                        t[s] = miss ? v : t[s];
                    }
#pragma unroll
                    for (int s = 0; s < 4; ++s) bad |= tag_miss(t[s], tag);
                    if (!__any(bad != 0)) break;
                }
            }
#pragma unroll
            for (int s = 0; s < 2; ++s)
                *(LAS v4u*)(L + a_l16 + (2 * wave + s) * 1024) =
                    (v4u){__builtin_amdgcn_perm(t[2 * s].y, t[2 * s].x, 0x07060302), __builtin_amdgcn_perm(t[2 * s].w, t[2 * s].z, 0x07060302),
                          __builtin_amdgcn_perm(t[2 * s + 1].y, t[2 * s + 1].x, 0x07060302), __builtin_amdgcn_perm(t[2 * s + 1].w, t[2 * s + 1].z, 0x07060302)};
        } else {
            asm volatile("s_barrier" ::: "memory");
            UK_ROWS();
        }
        wait_vm<0>();
        v4u uf[4];
#pragma unroll
        for (int s = 0; s < 4; ++s) uf[s] = lds128(L, a_uk[s]);
        barrier_lds();
        {
            v4u qf[4];
#pragma unroll
            for (int s = 0; s < 4; ++s) qf[s] = lds128(L, a_l16 + s * 1024);
            fence_sched();
            if (tid == 448) st64(ws, tag, WS_P2 + a * 128);
            f32x4 c = {0.f, 0.f, 0.f, 0.f};
#pragma unroll
            for (int s = 0; s < 4; ++s) c = mfma32(uf[s], qf[s], c);
            constexpr float SC = 0.08838834764831845f * 1.4426950408889634f;
#pragma unroll
            for (int r = 0; r < 4; ++r) { c[r] += dpp<XOR2>(c[r]); c[r] += dpp<ROR4>(c[r]); c[r] *= SC; }
            st64(ws, (v2u){pk(c[0], c[1]), pk(c[2], c[3])}, o_qa);
        }
        float obias = 0.f;
        if (wave == 6 && bid < 36) { const float gt = gate_of(xv, gv); obias = bfr(1.f - gt) * bflo(b2v); pin(obias); *(LAS f32x2*)(L + LA_G) = (f32x2){gt, bfr(1.f - gt)}; }
        if (tid < 20) *(LAS unsigned*)(L + LA_ZERO + 4 * tid) = 0u;
        wait_until(t0 + A_T);
        if (lane < 54) {
#pragma unroll
            for (int i = 0; i < 6; ++i) dma16(srd_w1, v_w1, (wave + 8 * i) * 27648, lds0 + LA_W1 + (wave + 8 * i) * 864);
        }
        asm volatile("s_barrier" ::: "memory");
        const int cs = j >> 1, ds = j & 1;
        const unsigned sub_c = 16 * ((lane & 7) ^ ((((lane >> 4) & 1) | ((wave & 1) << 1)) << 1));
        {
            const v4i srd = mk_srd(wuv + (size_t)cs * 256 * 4096 + h * 128 + ds * 64, 255 * 8192 + 128);
#pragma unroll
            for (int i = 0; i < 4; ++i) dma16(srd, (lane >> 3) * 8192 + sub_c, (wave + 8 * i) * 65536, lds0 + LA_WUV + (wave + 8 * i) * 1024);
        }
        const v4i srd_wo = mk_srd(wo + (size_t)(h * 128 + ds * 64) * 576, 64 * 1152);
        unsigned v_wo = (lane >> 3) * 1152 + sub_c;
        pin(v_wo);
#define WO_PIECE(i) do { const int G = (i) + 5 * cs - ((i) + 5 * cs >= 9 ? 9 : 0); dma16(srd_wo, v_wo, wave * 9216 + G * 128, lds0 + LA_WO + wave * 9216 + G * 1024); } while (0)
        if (WO_A) asm volatile("s_barrier" ::: "memory");
#pragma unroll
        for (int i = 0; i < WO_A; ++i) WO_PIECE(i);
        wait_vm<4 + WO_A>();
        barrier_lds();
        {
            const unsigned a0 = LA_W1 + (8 * g + q) * 288 + 8 * p + 32 * wave, at = LA_W1 + (128 + 4 * g + q) * 288 + 8 * p + 32 * wave;
#pragma unroll
            for (int t = 0; t < 2; ++t) {
                if (t == 0 || wave == 2) {
                    f32x4 c = {0.f, 0.f, 0.f, 0.f};
#pragma unroll
                    for (int s = 0; s < 4; ++s) c = mfma32(lds_tr2(L, a0 + t * 192 + s * 9216, a0 + t * 192 + s * 9216 + 1152), xf[s], c);
                    c = mfma16(lds_tr(L, at + t * 192), xt, c);
                    const v4u v = {__float_as_uint(c[0]), __float_as_uint(c[1]), __float_as_uint(c[2]), __float_as_uint(c[3])};
                    if (n < 2) st128(ws, v, WS_M1 + a * 1152 + n * 576 + (16 * wave + 96 * t + 4 * g) * 4);
                }
            }
        }
        if (WO_B > WO_A) asm volatile("s_barrier" ::: "memory");
#pragma unroll
        for (int i = WO_A; i < WO_B; ++i) WO_PIECE(i);
        const int b = wave >> 2, hf = lane >> 5, li = lane & 31, k0 = 32 * b + 8 * (wave & 3) + 4 * hf, col = 4 * (wave & 3) + 2 * hf + b;
        unsigned o_h3 = WS_H3 + k0 * 32768 + h * 1024 + cs * 512 + li * 16, o_ml = WS_ML + (k0 + p) * 256 + h * 8;
        unsigned a_cw = LA_CMB + col * 512 + 16 * (li ^ col), a_col = LA_COL + 8 * col, a_colr = LA_COL + 8 * n;
        unsigned a_uv = LA_WUV + g * 1024 + q * 128 + 16 * ((2 * (wave & 3) + (p >> 1)) ^ (((q >> 1) | ((g & 1) << 1)) << 1)) + 8 * (p & 1);
        unsigned a_cr[4], a_c[5], o5 = WS_H5 + ((lane >> 3) + 8 * wave) * 4608 + bid * 128 + (lane & 7) * 16;
#pragma unroll
        for (int s = 0; s < 4; ++s) { a_cr[s] = LA_CMB + n * 512 + 16 * ((4 * s + g) ^ n); pin(a_cr[s]); }
        unsigned a_w = LA_WO + g * 9216 + (wave >> 2) * 1024 + q * 128 + 16 * ((2 * (wave & 3) + (p >> 1)) ^ (((q >> 1) | ((g & 1) << 1)) << 1)) + 8 * (p & 1), a_w2 = a_w + 36864;
#pragma unroll
        for (int i = 0; i < 5; ++i) { a_c[i] = (n >> 1) == i ? LA_CTX + (n & 1) * 128 + 16 * g : LA_ZERO; pin(a_c[i]); }
        unsigned a_cxw = LA_CTX + (n & 1) * 128 + (16 * wave + 4 * g) * 2;
        unsigned o_h5 = (n >> 1) < 5 - (wave >> 2) ? WS_H5 + bid * 4608 + (n & 1) * 2304 + ((wave >> 2) * 64 + (wave & 3) * 16 + 4 * g) * 4 + (n >> 1) * 512 : NOWHERE;
        unsigned a_rw = LA_RED + ((lane & 7) * 4 + 2 * hf + (g & 1)) * 32 + wave * 4, a_rr = LA_RED + li * 32;
        pin(o_h3); pin(o_ml); pin(a_cw); pin(a_col); pin(a_colr); pin(o5); pin(a_rw); pin(a_rr); pin(a_uv); pin(a_w); pin(a_w2); pin(a_cxw); pin(o_h5);
        if (wave == 0 || (wave == 1 && bid < 36)) wait_flags3<6>(wsd, WS_R3 + (((h >> 4) ^ wave) * 64 + lane) * 128, tag);
        wait_vm<1 + WO_B>();
        asm volatile("s_barrier" ::: "memory");
        v4u r[4]; v2u ml;
#pragma unroll
        for (int i = 0; i < 4; ++i) asm volatile("buffer_load_dwordx4 %0, %1, %2, %3 offen sc1" : "=v"(r[i]) : "v"(o_h3), "s"(wsd), "s"(i * 32768) : "memory");
        asm volatile("buffer_load_dwordx2 %0, %1, %2, 0 offen sc1" : "=v"(ml) : "v"(o_ml), "s"(wsd) : "memory");
        if (WO_B < 9) asm volatile("s_barrier" ::: "memory");
#pragma unroll
        for (int i = WO_B; i < 9; ++i) WO_PIECE(i);
        v4u vf[8];
        if (wave < 4) {
#pragma unroll
            for (int s = 0; s < 8; ++s) vf[s] = lds_tr2(L, a_uv + s * 4096, a_uv + s * 4096 + 512);
        }
        asm volatile("s_waitcnt vmcnt(%5)" : "+v"(r[0]), "+v"(r[1]), "+v"(r[2]), "+v"(r[3]), "+v"(ml) : "i"(9 - WO_B) : "memory");
#define H3_ALL() umax2(umax4((v4u){umax4(r[0]), umax4(r[1]), umax4(r[2]), umax4(r[3])}), umax2(ml.x, ml.y))
        if (__builtin_expect(__any(H3_ALL() == SENT), 0)) {
#pragma unroll 1
            for (unsigned spins = 0; spins < SPIN_CAP; ++spins) {
                asm volatile("" ::: "memory");
#pragma unroll
                for (int i = 0; i < 4; ++i) { const bool m = umax4(r[i]) == SENT; const v4u v = ld128(ws, m ? o_h3 + i * 32768 : NOWHERE); r[i] = m ? v : r[i]; }
                const bool m2 = umax2(ml.x, ml.y) == SENT;
                const v2u v2 = ld64(ws, m2 ? o_ml : NOWHERE);
                ml = m2 ? v2 : ml;
                if (!__any(H3_ALL() == SENT)) break;
            }
        }
        {
            float mx = fmx(__uint_as_float(ml.x), dpp<XOR1>(__uint_as_float(ml.x))); mx = fmx(mx, dpp<XOR2>(mx));
            const float w = __builtin_amdgcn_exp2f(__uint_as_float(ml.x) - mx);
            const float wk[4] = {dpp<0x00>(w), dpp<0x55>(w), dpp<0xAA>(w), dpp<0xFF>(w)};
            float acc[8];
#pragma unroll
            for (int k = 0; k < 4; ++k) { acc[2 * k] = wk[0] * bflo(r[0][k]); acc[2 * k + 1] = wk[0] * bfhi(r[0][k]); }
#pragma unroll
            for (int i = 1; i < 4; ++i)
#pragma unroll
                for (int k = 0; k < 4; ++k) { acc[2 * k] = fmaf(wk[i], bflo(r[i][k]), acc[2 * k]); acc[2 * k + 1] = fmaf(wk[i], bfhi(r[i][k]), acc[2 * k + 1]); }
            *(LAS v4u*)(L + a_cw) = (v4u){pk(acc[0], acc[1]), pk(acc[2], acc[3]), pk(acc[4], acc[5]), pk(acc[6], acc[7])};
            float ls = w * __uint_as_float(ml.y); ls += dpp<XOR1>(ls); ls += dpp<XOR2>(ls);
            *(LAS f32x2*)(L + a_col) = (f32x2){mx, ls};
        }
        wait_vm<0>();
        barrier_lds();
        v4u wf[5][2];
#define WO_FRAGS() do { _Pragma("unroll") for (int i = 0; i < 5; ++i) if (i < 4 || wave < 4) { \
            wf[i][0] = lds_tr2(L, a_w + i * 2048, a_w + i * 2048 + 512); wf[i][1] = lds_tr2(L, a_w2 + i * 2048, a_w2 + i * 2048 + 512); } } while (0)
        if (wave < 4) {
            const f32x2 cm = *(LAS f32x2*)(L + a_colr);
            v4u cf[8];
#pragma unroll
            for (int s = 0; s < 8; ++s) cf[s] = lds128(L, a_cr[s & 3] + 256 * (s >> 2));
            fence_sched();
            f32x4 c = {0.f, 0.f, 0.f, 0.f};
#pragma unroll
            for (int s = 0; s < 8; ++s) c = mfma32(vf[s], cf[s], c);
            float mx = fmx(cm[0], dpp<XOR2>(cm[0])); mx = fmx(mx, dpp<ROR4>(mx)); mx = fmx(mx, dpp<ROR8>(mx));
            const float wc = __builtin_amdgcn_exp2f(cm[0] - mx);
            float ls = wc * cm[1]; ls += dpp<XOR2>(ls); ls += dpp<ROR4>(ls); ls += dpp<ROR8>(ls);
            const float sc = wc * __builtin_amdgcn_rcpf(ls);
            fence_sched();
            WO_FRAGS();
            fence_sched();
#pragma unroll
            for (int k = 0; k < 4; ++k) { c[k] *= sc; c[k] += dpp<XOR2>(c[k]); c[k] += dpp<ROR4>(c[k]); c[k] += dpp<ROR8>(c[k]); }
            *(LAS v2u*)(L + a_cxw) = (v2u){pk(c[0], c[1]), pk(c[2], c[3])};
        } else WO_FRAGS();
        barrier_lds();
        {
            v4u f0[5], f1[5];
#pragma unroll
            for (int i = 0; i < 5; ++i) if (i < 4 || wave < 4) { f0[i] = lds128(L, a_c[i]); f1[i] = lds128(L, a_c[i] + 64); }
            fence_sched();
            f32x4 o = {0.f, 0.f, 0.f, 0.f};
#pragma unroll
            for (int i = 0; i < 5; ++i) if (i < 4 || wave < 4) o = mfma32(wf[i][1], f1[i], mfma32(wf[i][0], f0[i], o));
            st128(ws, (v4u){__float_as_uint(o[0]), __float_as_uint(o[1]), __float_as_uint(o[2]), __float_as_uint(o[3])}, o_h5);
        }
        if (bid < 36) {
            const f32x2 gg = *(LAS f32x2*)(L + LA_G);
            if (H5_DELAY) __builtin_amdgcn_s_sleep(H5_DELAY);
            v4u r5[4];
            const unsigned off[4] = {o5 + 128 * 4608, o5 + 192 * 4608, o5, o5 + 64 * 4608};
#pragma unroll
            for (int i = 0; i < 4; ++i) r5[i] = ld128(ws, off[i]);
            if (!landed<4>(r5)) regather<4>(ws, off, r5);
            float t[4];
#pragma unroll
            for (int k = 0; k < 4; ++k) t[k] = fmaf(gg[0], __uint_as_float(r5[2][k]) + __uint_as_float(r5[3][k]), gg[1] * (__uint_as_float(r5[0][k]) + __uint_as_float(r5[1][k])));
            auto s0 = __builtin_amdgcn_permlane32_swap(__float_as_uint(t[0]), __float_as_uint(t[2]), false, false);
            auto s1 = __builtin_amdgcn_permlane32_swap(__float_as_uint(t[1]), __float_as_uint(t[3]), false, false);
            const float u0 = __uint_as_float(s0[0]) + __uint_as_float(s0[1]), u1 = __uint_as_float(s1[0]) + __uint_as_float(s1[1]);
            auto s2 = __builtin_amdgcn_permlane16_swap(__float_as_uint(u0), __float_as_uint(u1), false, false);
            float v = __uint_as_float(s2[0]) + __uint_as_float(s2[1]);
            v += dpp<ROR8>(v);
            *(LAS float*)(L + a_rw) = v;
            barrier_lds();
            if (wave == 6) {
                const f32x4 x0 = *(LAS f32x4*)(L + a_rr), x1 = *(LAS f32x4*)(L + a_rr + 16);
                const float res = (((x0[0] + x0[1]) + (x0[2] + x0[3])) + ((x1[0] + x1[1]) + (x1[2] + x1[3]))) + obias;
                __builtin_amdgcn_raw_buffer_store_b16((u16)pk(res, res), __builtin_amdgcn_make_buffer_rsrc(out, 0, 2304, 0x00020000), lane < 32 ? bid * 64 + lane * 2 : NOWHERE, 0, 0);
                st64(ws, tag, bid == 0 && lane == 0 ? WS_E : NOWHERE);
            }
        }
    } else {
        const int sb = slot - 16, chunk = xcd * 8 + (sb >> 1), hh = sb & 1, b = chunk >> 5, bi = hh * 64 + chunk;
        asm volatile("s_nop 4" ::: "memory");
        v2u e = {0u, 0u};
        if (wave == 0) ald_e(e, wsd);
        wait_until(t0 + PF_T);
        if (tid < 256) touch(mk_srd(wuk, 512 * 8192), (sb * 32 + (tid >> 3)) * 8192 + xcd * 1024 + (tid & 7) * 128);
        asm volatile("s_barrier" ::: "memory");
        v4u fa = {0u, 0u, 0u, 0u};
        const v4i srd_c = mk_srd(ckv + (size_t)chunk * 128 * 512, 131072);
#define CKV_ROW(i) do { const int r = (((i) + wave) & 7) + 8 * (((i) >> 3) ^ hh); \
            dma16(srd_c, (16 * lane) ^ (16 * (2 * (r & 7) + (r >> 3))), (16 * wave + r) * 1024, lds0 + LB_CKV + (16 * wave + r) * 1024); } while (0)
#pragma unroll
        for (int i = 0; i < 8; ++i) CKV_ROW(i);
        fence_sched();
        unsigned o_c3 = WS_H3 + chunk * 32768 + hh * 16384 + tid * 16;
        unsigned o_c5 = tid < 288 ? WS_H5 + bid * 4608 + tid * 16 : tid < 296 ? WS_ML + chunk * 256 + hh * 128 + (tid - 288) * 16 : NOWHERE;
        if (tid == 0) *(LAS unsigned*)(L + LB_CNT) = 0u;
        const int fsw = 2 * (n & 7) + (n >> 3), pg = ((g & 1) << 1) | (g >> 1), rr = 4 * g + q, fr = 2 * (rr & 7) + (rr >> 3);
        unsigned a_k[4], a_qr[4], a_v[4][2];
#pragma unroll
        for (int s = 0; s < 4; ++s) {
            a_qr[s] = LB_Q + n * 1024 + 16 * ((4 * s + pg) ^ fsw); a_k[s] = a_qr[s] - LB_Q + LB_CKV + wave * 16384;
            a_v[s][0] = LB_CKV + rr * 1024 + 256 * (wave >> 1) + 16 * ((8 * (wave & 1) + 2 * s + (p >> 1)) ^ fr) + 8 * (p & 1); a_v[s][1] = a_v[s][0] + 65536;
            pin(a_qr[s]); pin(a_k[s]); pin(a_v[s][0]); pin(a_v[s][1]);
        }
        unsigned o_qa = WS_QA + b * 32768 + hh * 16384 + tid * 16, a_qw0 = LB_Q + wave * 1024 + 16 * (lane ^ (2 * wave)), a_qw1 = LB_Q + (wave + 8) * 1024 + 16 * (lane ^ (2 * wave + 1));
        unsigned a_mx = LB_MX + n * 32, a_mw = LB_MX + n * 32 + wave * 4, a_pw = LB_P + (wave >> 1) * 1024 + lane * 16 + (wave & 1) * 8, a_pr = LB_P + lane * 16;
        unsigned o_h3 = WS_H3 + chunk * 32768 + (hh * 16 + (n & 7)) * 1024 + wave * 128 + (n >> 3) * 64 + pg * 16;
        unsigned o_p1 = WS_P1 + (hh * 64 + lane) * 128, o_ml = g == 0 ? WS_ML + chunk * 256 + (hh * 16 + n) * 8 : NOWHERE, o_h3b = o_h3 + 8192;
        pin(o_ml); pin(o_h3b);
        pin(o_qa); pin(a_qw0); pin(a_qw1); pin(a_mx); pin(a_mw); pin(a_pw); pin(a_pr); pin(o_h3); pin(o_p1); pin(o_c3); pin(o_c5);
        unsigned o_c3b = o_c3 + 8192; pin(o_c3b);
        const v4i srd_2 = mk_srd(w2 + (size_t)bi * 36 * 576, 36 * 1152);
        unsigned v_w = (lane >> 3) * 1152 + 16 * ((lane & 7) ^ ((((lane >> 4) & 1) | ((wave & 1) << 1)) << 1));
        unsigned a_w = LB_W + g * 9216 + (wave >> 1) * 3072 + q * 128 + 16 * ((4 * (wave & 1) + (p >> 1)) ^ (((q >> 1) | ((g & 1) << 1)) << 1)) + 8 * (p & 1), a_w1 = a_w ^ 32;
        unsigned a_wt = LB_W + 4 * 9216 + (wave >> 1) * 3072 + q * 128 + 16 * ((4 * (wave & 1) + (p >> 1)) ^ ((q >> 1) << 1)) + 8 * (p & 1), a_wt1 = a_wt ^ 32;
        unsigned o_h5 = n < 12 ? WS_H5 + bid * 4608 + (n & 1) * 2304 + ((3 * (wave >> 1) + (n >> 2)) * 64 + (2 * (wave & 1) + ((n >> 1) & 1)) * 16 + 4 * g) * 4 : NOWHERE;
        unsigned a_h = LB_H + (n & 1) * 256 + 16 * g, a_ht = LB_H + (n & 1) * 256 + 64 + 8 * g;
        unsigned o1 = WS_M1 + (bi & ~3) * 1152 + (wave & 1) * 576 + ((bi & 3) * 36 + (lane < 36 ? lane : 35)) * 4;
        pin(v_w); pin(a_w); pin(a_w1); pin(a_wt); pin(a_wt1); pin(o_h5); pin(a_h); pin(a_ht); pin(o1);
        v4u m1 = {0u, 0u, 0u, 0u};
        unsigned b1v = 0;
        if (wave >= 6) asm volatile("buffer_load_ushort %0, %1, %2, 0 offen" : "+v"(b1v) : "v"(lane * 2), "s"(mk_srd(b1 + bi * 36, 72)) : "memory");
        constexpr int NG = 9 - 2 * WO_B > 0 ? 9 - 2 * WO_B : 0;
        unsigned o_pfo = NOWHERE;
        if (NG != 0) { const int t = tid < 32 * NG ? tid : 0, r = t / (NG ? NG : 1), k = t % (NG ? NG : 1); o_pfo = tid < 32 * NG ? (xcd * 512 + sb * 32 + r) * 1152 + (k < 5 - WO_B ? WO_B + k : 2 * WO_B + k) * 128 : NOWHERE; }
        pin(o_pfo);
        pin(m1);
        asm volatile("s_barrier" ::: "memory");
        v2u tag = {0u, 0u};
        u64 t_q = 0;
        bool late = false;
#pragma unroll
        for (int i = 8; i < 16; ++i) {
            if (i == FA_AT && wave == 0) asm volatile("buffer_load_dwordx4 %0, %1, %2, 0 offen sc1" : "+v"(fa) : "v"(o_p1), "s"(wsd) : "memory");
            if (i == FA_USE && wave == 0) {
                asm volatile("s_waitcnt vmcnt(%2)" : "+v"(fa), "+v"(e) : "i"(FA_USE - FA_AT) : "memory");
                tag = mk_tag(e);
                late = !flags_ok((v2u){fa.x, fa.y}, tag);
                int d = (int)(fa.z - (unsigned)t0);
                d = max(d, __builtin_amdgcn_mov_dpp(d, XOR1, 0xf, 0xf, true)); d = max(d, __builtin_amdgcn_mov_dpp(d, XOR2, 0xf, 0xf, true));
                d = max(d, __builtin_amdgcn_mov_dpp(d, ROR4, 0xf, 0xf, true)); d = max(d, __builtin_amdgcn_mov_dpp(d, ROR8, 0xf, 0xf, true));
                d = max(max(__builtin_amdgcn_readlane(d, 0), __builtin_amdgcn_readlane(d, 16)), max(__builtin_amdgcn_readlane(d, 32), __builtin_amdgcn_readlane(d, 48)));
                t_q = t0 + min(max(d, 0), QA_T + 200);
                asm volatile("s_nop 4" ::: "memory");
            }
            CKV_ROW(i);
        }
        if (wave == 0) {
            if (__builtin_expect(late, 0)) {
                wait_flags<4>(ws, o_p1, tag);
                wait_flags<0>(ws, o_p1 + (WS_P2 - WS_P1), tag);
            } else wait_until(t_q);
        }
        asm volatile("s_barrier" ::: "memory");
        v4u kf[16];
        {
            v4u r[2];
            const unsigned off[2] = {o_qa, o_qa + 8192};
            r[0] = ald128(wsd, off[0]); r[1] = ald128(wsd, off[1]);
            asm volatile("s_barrier" ::: "memory");
            st128(ws, CLEAN, o_c3); st128(ws, CLEAN, o_c3b); st128(ws, CLEAN, o_c5);
            WAIT_VM(5, "+v"(b1v));
#pragma unroll
            for (int s = 0; s < 16; ++s) kf[s] = lds128(L, a_k[s & 3] + 256 * (s >> 2));
            WAIT_VM(3, "+v"(r[0]), "+v"(r[1]));
            if (__builtin_expect(!landed<2>(r), 0)) regather<2, 2>(ws, off, r);
            *(LAS v4u*)(L + a_qw0) = r[0]; *(LAS v4u*)(L + a_qw1) = r[1];
        }
        barrier_lds();
        fence_sched();
        f32x4 sc = {0.f, 0.f, 0.f, 0.f};
        v4u vf[16];
#define V_FRAG(i) vf[i] = lds_tr2(L, a_v[(i) >> 2][((i) >> 1) & 1] + ((i) & 1) * 32768, a_v[(i) >> 2][((i) >> 1) & 1] + ((i) & 1) * 32768 + 16384)
        {
            v4u qf[16];
#pragma unroll
            for (int s = 0; s < 16; ++s) qf[s] = lds128(L, a_qr[s & 3] + 256 * (s >> 2));
            fence_sched();
#pragma unroll
            for (int s = 0; s < 16; ++s) sc = mfma32(kf[s], qf[s], sc);
        }
        *(LAS float*)(L + a_mw) = x32max(x16max(fmx(fmx(sc[0], sc[1]), fmx(sc[2], sc[3]))));
        wait_vm<0>();
        barrier_lds();
        if (tid == 0) st64(ws, tag, WS_R3 + bi * 128);
        float mx;
        {
            const f32x4 m0 = *(LAS f32x4*)(L + a_mx), m1 = *(LAS f32x4*)(L + a_mx + 16);
            fence_sched();
#pragma unroll
            for (int i = 0; i < TR_EARLY; ++i) V_FRAG(i);
            fence_sched();
            mx = fmx(fmx(fmx(m0[0], m0[1]), fmx(m0[2], m0[3])), fmx(fmx(m1[0], m1[1]), fmx(m1[2], m1[3])));
            const float p0 = __builtin_amdgcn_exp2f(sc[0] - mx), p1 = __builtin_amdgcn_exp2f(sc[1] - mx), p2 = __builtin_amdgcn_exp2f(sc[2] - mx), p3 = __builtin_amdgcn_exp2f(sc[3] - mx);
            *(LAS v2u*)(L + a_pw) = (v2u){pk(p0, p1), pk(p2, p3)};
            *(LAS float*)(L + a_mw + (LB_L - LB_MX)) = x32sum(x16sum((p0 + p1) + (p2 + p3)));
        }
        barrier_lds();
        {
            if (wave < 4) __builtin_amdgcn_s_setprio(1);
            v4u pf[4];
#pragma unroll
            for (int s = 0; s < 4; ++s) pf[s] = lds128(L, a_pr + s * 1024);
            const f32x4 l0 = *(LAS f32x4*)(L + a_mx + (LB_L - LB_MX)), l1 = *(LAS f32x4*)(L + a_mx + (LB_L - LB_MX) + 16);
            fence_sched();
#pragma unroll
            for (int i = TR_EARLY; i < 16; ++i) V_FRAG(i);
            fence_sched();
            if (wave == 7) st64(ws, (v2u){__float_as_uint(mx), __float_as_uint(((l0[0] + l0[1]) + (l0[2] + l0[3])) + ((l1[0] + l1[1]) + (l1[2] + l1[3])))}, o_ml);
            v4u o[2];
#pragma unroll
            for (int u = 0; u < 2; ++u) {
                f32x4 c0 = {0.f, 0.f, 0.f, 0.f}, c1 = {0.f, 0.f, 0.f, 0.f};
#pragma unroll
                for (int s = 0; s < 4; ++s) c0 = mfma32(vf[8 * u + s], pf[s], c0);
#pragma unroll
                for (int s = 0; s < 4; ++s) c1 = mfma32(vf[8 * u + 4 + s], pf[s], c1);
                auto s0 = __builtin_amdgcn_permlane16_swap(pk(c0[0], c0[1]), pk(c1[0], c1[1]), false, false);
                auto s1 = __builtin_amdgcn_permlane16_swap(pk(c0[2], c0[3]), pk(c1[2], c1[3]), false, false);
                o[u] = (v4u){s0[0], s1[0], s0[1], s1[1]};
            }
            v4u x0, x1;
#pragma unroll
            for (int k = 0; k < 4; ++k) {
                x0[k] = __builtin_amdgcn_update_dpp(o[0][k], o[1][k], ROR8, 0xf, 0xc, false);
                x1[k] = __builtin_amdgcn_update_dpp(o[1][k], o[0][k], ROR8, 0xf, 0x3, false);
            }
            st128(ws, x0, o_h3); st128(ws, x1, o_h3b);
            __builtin_amdgcn_s_setprio(0);
        }
        if (wave >= 6) {
#pragma unroll
            for (int k = 0; k < 4; ++k) asm volatile("buffer_load_dword %0, %1, %2, 0 offen offset:%3 sc1" : "+v"(m1[k]) : "v"(o1), "s"(wsd), "i"(k * 1152) : "memory");
        }
        barrier_lds();
        if (NG != 0) { touch(mk_srd(wo, 4096 * 1152), o_pfo); asm volatile("s_barrier" ::: "memory"); }
        if (wave < 6) {
            if (W_DELAY) __builtin_amdgcn_s_sleep(W_DELAY);
            const int third = wave >> 1;
            const unsigned so = (wave & 1) * 9216 + third * 384, lo = lds0 + LB_W + (wave & 1) * 9216 + third * 3072;
            if (!(wave & 1)) {
#pragma unroll
                for (int i = 0; i < 3; ++i) dma16(srd_2, v_w, so + 4 * 9216 + i * 128, lo + 4 * 9216 + i * 1024);
            }
#pragma unroll
            for (int i = 0; i < 6; ++i) dma16(srd_2, v_w, so + (i / 3) * 18432 + (i % 3) * 128, lo + (i / 3) * 18432 + (i % 3) * 1024);
            wait_vm<0>();
            lds_arrive(L, LB_CNT, lane);
            lds_wait(L, LB_CNT, 8);
            v4u wm[6]; v2u wt[6];
            const v4u f1 = lds128(L, a_h);
            const v2u ft = *(LAS v2u*)(L + a_ht);
#pragma unroll
            for (int i = 0; i < 6; ++i) {
                const unsigned aw = (i & 1) ? a_w1 : a_w, at = (i & 1) ? a_wt1 : a_wt, o = (i >> 1) * 1024;
                wm[i] = lds_tr2(L, aw + o, aw + o + 512); wt[i] = lds_tr(L, at + o);
            }
            fence_sched();
            f32x4 sel;
#pragma unroll
            for (int i = 0; i < 6; ++i) {
                const f32x4 c = mfma16(wt[i], ft, mfma32(wm[i], f1, (f32x4){0.f, 0.f, 0.f, 0.f}));
#pragma unroll
                for (int k = 0; k < 4; ++k) sel[k] = (i == 0 || (n >> 1) == i) ? c[k] : sel[k];
            }
            st128(ws, (v4u){__float_as_uint(sel[0]), __float_as_uint(sel[1]), __float_as_uint(sel[2]), __float_as_uint(sel[3])}, o_h5);
        } else {
            WAIT_VM(0, "+v"(m1));
#pragma unroll 1
            for (unsigned spins = 0; spins < SPIN_CAP && __any(umax4(m1) == SENT); ++spins) {
                asm volatile("" ::: "memory");
                m1 = (v4u){ld32(ws, o1), ld32(ws, o1 + 1152), ld32(ws, o1 + 2304), ld32(ws, o1 + 3456)};
            }
            const float v = bfr(bfr((__uint_as_float(m1.x) + __uint_as_float(m1.y)) + (__uint_as_float(m1.z) + __uint_as_float(m1.w))) + bflo(b1v));
            const float u = 0.7978845608028654f * (v + 0.044715f * v * v * v);
            const float hv = v / (1.f + __expf(-2.f * u));
            if (lane < 48) *(LAS u16*)(L + LB_H + (wave & 1) * 256 + lane * 2) = lane < 36 ? (u16)pk(hv, hv) : (u16)0;
            asm volatile("s_waitcnt lgkmcnt(0)" ::: "memory");
            lds_arrive(L, LB_CNT, lane);
        }
    }
}

extern "C" void kernel_launch(void* const* d_in, const int* in_sizes, int n_in, void* d_out, int out_size, void* d_ws, size_t ws_size, hipStream_t stream) {
    fused_decode_kernel<<<256, NT, 0, stream>>>((const u16*)d_in[0], (const u16*)d_in[1], (const u16*)d_in[2], (const u16*)d_in[3], (const u16*)d_in[4],
                                                (const u16*)d_in[5], (const u16*)d_in[7], (const u16*)d_in[8], (const u16*)d_in[9], (const u16*)d_in[10],
                                                (const u16*)d_in[11], (u16*)d_out, (char*)d_ws);
}
```
